```python
import torch

def _softmax(x, axis=-1):
    return torch.softmax(x, dim=axis)
def _gelu(x):
    return torch.nn.functional.gelu(x, approximate="tanh")
def _silu(x):
    return torch.nn.functional.silu(x)
def _sigmoid(x):
    return torch.sigmoid(x)
def _layernorm(x, axis=-1, eps=1e-5):
    m = x.mean(dim=axis, keepdim=True)
    v = x.var(dim=axis, keepdim=True, unbiased=False)
    return (x - m) / torch.sqrt(v + eps)
def _scatter_add(tgt, idx, src, axis=0):
    return tgt.clone().index_add_(axis, idx, src)
def _segment_sum(src, seg, n):
    out = torch.zeros((n,) + src.shape[1:], dtype=src.dtype)
    return out.index_add_(0, seg, src)
def _pad(x, pw):
    flat = tuple(v for pair in reversed(pw) for v in pair)
    return torch.nn.functional.pad(x, flat)

kernel_name = 'concat_heads__mha_block_cached__gqa_block_cached__near_ridge'

def setup_inputs():

    _w = (lambda a: (lambda t: t.to(torch.bfloat16) if t.is_floating_point() else t)(torch.as_tensor(a)))
    def _A_setup():
        B, Sq, D = 16, 32, 1024
        H, Sk, Dh = 32, 32, 128
        return {
            "x": _w((lambda *s: torch.randn(*s, dtype=torch.bfloat16) * 0.8)(B, Sq, D)),
            "k_cache": _w((lambda *s: torch.randn(*s, dtype=torch.bfloat16) * 0.8)(B, H, Sk, Dh)),
            "v_cache": _w((lambda *s: torch.randn(*s, dtype=torch.bfloat16) * 0.05)(B, H, Sk, Dh)),
            "wq": _w((lambda *s: torch.randn(*s, dtype=torch.bfloat16) * 0.8)(D, H * Dh) * (D ** -0.5)),
            "wo": _w((lambda *s: torch.randn(*s, dtype=torch.bfloat16) * 0.05)(H * Dh, D) * ((H * Dh) ** -0.5)),
        }
    _A_in = _A_setup()

    def _B_setup():
        B, Sq, D = 16, 32, 1024
        Hq, Hkv, Sk, Dh = 32, 32, 32, 128
        assert Hq % Hkv == 0, (Hq, Hkv)
        return {
            "x": _w((lambda *s: torch.randn(*s, dtype=torch.bfloat16) * 0.8)(B, Sq, D)),
            "k_cache": _w((lambda *s: torch.randn(*s, dtype=torch.bfloat16) * 0.8)(B, Hkv, Sk, Dh)),
            "v_cache": _w((lambda *s: torch.randn(*s, dtype=torch.bfloat16) * 0.05)(B, Hkv, Sk, Dh)),
            "wq": _w((lambda *s: torch.randn(*s, dtype=torch.bfloat16) * 0.8)(D, Hq * Dh) * (D ** -0.5)),
            "wo": _w((lambda *s: torch.randn(*s, dtype=torch.bfloat16) * 0.05)(Hq * Dh, D) * ((Hq * Dh) ** -0.5)),
        }
    _B_in = _B_setup()

    return {
        "A_x": _A_in["x"],
        "A_k_cache": _A_in["k_cache"],
        "A_v_cache": _A_in["v_cache"],
        "A_wq": _A_in["wq"],
        "A_wo": _A_in["wo"],
        "B_x": _B_in["x"],
        "B_k_cache": _B_in["k_cache"],
        "B_v_cache": _B_in["v_cache"],
        "B_wq": _B_in["wq"],
        "B_wo": _B_in["wo"],
    }

def reference(A_x, A_k_cache, A_v_cache, A_wq, A_wo, B_x, B_k_cache, B_v_cache, B_wq, B_wo):
    def _A_ref(x, k_cache, v_cache, wq, wo):
        B, Sq, D = x.shape
        H, Dh = 32, 128
        q = (lambda x, *d: x.permute(*d))((x @ wq).reshape(B, Sq, H, Dh), 0, 2, 1, 3)
        scores = torch.einsum("bhqd,bhkd->bhqk", q, k_cache) * (Dh ** -0.5)
        p = _softmax(scores, axis=-1)
        o = torch.einsum("bhqk,bhkd->bhqd", p, v_cache)
        return (lambda x, *d: x.permute(*d))(o, 0, 2, 1, 3).reshape(B, Sq, H * Dh) @ wo

    def _B_ref(x, k_cache, v_cache, wq, wo):
        B, Sq, D = x.shape
        Hq, Dh = 32, 128
        Hkv = k_cache.shape[1]
        rep = Hq // Hkv
        q = (lambda x, *d: x.permute(*d))((x @ wq).reshape(B, Sq, Hq, Dh), 0, 2, 1, 3)
        k = (lambda x, n, axis: torch.repeat_interleave(x, n, dim=axis))(k_cache, rep, axis=1)
        v = (lambda x, n, axis: torch.repeat_interleave(x, n, dim=axis))(v_cache, rep, axis=1)
        scores = torch.einsum("bhqd,bhkd->bhqk", q, k) * (Dh ** -0.5)
        p = _softmax(scores, axis=-1)
        o = torch.einsum("bhqk,bhkd->bhqd", p, v)
        return (lambda x, *d: x.permute(*d))(o, 0, 2, 1, 3).reshape(B, Sq, Hq * Dh) @ wo

    a = _A_ref(A_x, A_k_cache, A_v_cache, A_wq, A_wo)
    b = _B_ref(B_x, B_k_cache, B_v_cache, B_wq, B_wo)
    return torch.concatenate([a, b], axis=-1)
```

```cpp
#include <hip/hip_runtime.h>
#include <hip/hip_bf16.h>
typedef unsigned short u16;
__device__ __forceinline__ float bf2f(u16 b){ return __builtin_bit_cast(float,(unsigned)b<<16); }
__device__ __forceinline__ u16 f2bf(float f){ __hip_bfloat16 h = __float2bfloat16(f); return __builtin_bit_cast(u16,h); }

__global__ void k_q(const u16* x, const u16* wq, u16* q){
  int n = blockIdx.x*256+threadIdx.x;
  int m = blockIdx.y;
  float acc=0;
  for(int k=0;k<1024;++k) acc += bf2f(x[m*1024+k])*bf2f(wq[k*4096+n]);
  q[m*4096+n]=f2bf(acc);
}
__global__ void k_attn(const u16* q, const u16* kc, const u16* vc, u16* o){
  int b=blockIdx.x/32,h=blockIdx.x%32;
  __shared__ float p[32][33];
  int t=threadIdx.x;
  for(int e=t;e<1024;e+=256){ int i=e/32,j=e%32; float acc=0;
    for(int d=0;d<128;++d) acc+=bf2f(q[(b*32+i)*4096+h*128+d])*bf2f(kc[((b*32+h)*32+j)*128+d]);
    float s = bf2f(f2bf(acc)); s = bf2f(f2bf(s*0.08838834764831845f));
    p[i][j]=s; }
  __syncthreads();
  if(t<32){ float mx=-1e30f; for(int j=0;j<32;++j) mx=fmaxf(mx,p[t][j]); float sum=0; for(int j=0;j<32;++j){ float e=__expf(p[t][j]-mx); p[t][j]=e; sum+=e;} for(int j=0;j<32;++j) p[t][j]=bf2f(f2bf(p[t][j]/sum)); }
  __syncthreads();
  for(int e=t;e<32*128;e+=256){ int i=e/128,d=e%128; float acc=0; for(int j=0;j<32;++j) acc+=p[i][j]*bf2f(vc[((b*32+h)*32+j)*128+d]); o[(b*32+i)*4096+h*128+d]=f2bf(acc);}
}
__global__ void k_o(const u16* o, const u16* wo, u16* out, int off){
  int n = blockIdx.x*256+threadIdx.x;
  int m = blockIdx.y;
  float acc=0;
  for(int k=0;k<4096;++k) acc += bf2f(o[m*4096+k])*bf2f(wo[k*1024+n]);
  out[m*2048+off+n]=f2bf(acc);
}
extern "C" void kernel_launch(void* const* d_in, const int* in_sizes, int n_in,
                              void* d_out, int out_size, void* d_ws, size_t ws_size,
                              hipStream_t stream) {
  for(int br=0;br<2;++br){
    const u16* x=(const u16*)d_in[br*5+0]; const u16* kc=(const u16*)d_in[br*5+1]; const u16* vc=(const u16*)d_in[br*5+2];
    const u16* wq=(const u16*)d_in[br*5+3]; const u16* wo=(const u16*)d_in[br*5+4];
    u16* q=(u16*)d_ws + (size_t)br*2*512*4096; u16* o=q+512*4096;
    k_q<<<dim3(16,512),256,0,stream>>>(x,wq,q);
    k_attn<<<512,256,0,stream>>>(q,kc,vc,o);
    k_o<<<dim3(4,512),256,0,stream>>>(o,wo,(u16*)d_out,br*1024);
  }
}
```

```cpp
#include <hip/hip_runtime.h>
#include <hip/hip_bf16.h>
typedef unsigned v4u __attribute__((ext_vector_type(4)));
typedef unsigned v2u __attribute__((ext_vector_type(2)));
typedef float f32x4 __attribute__((ext_vector_type(4)));
typedef unsigned short u16;
#define GAS __attribute__((address_space(1)))
#define LAS __attribute__((address_space(3)))
#define M16(c,a,b) asm volatile("v_mfma_f32_16x16x32_bf16 %0, %1, %2, %0":"+v"(c):"v"(a),"v"(b))
#define RD128(d,addr,off) asm volatile("ds_read_b128 %0, %1 offset:%2":"=v"(d):"v"(addr),"i"(off))
#define RD64(d,addr,off) asm volatile("ds_read_b64 %0, %1 offset:%2":"=v"(d):"v"(addr),"i"(off))
#define RDTR(d,addr,off) asm volatile("ds_read_b64_tr_b16 %0, %1 offset:%2":"=v"(d):"v"(addr),"i"(off))
#define WAITL(n) asm volatile("s_waitcnt lgkmcnt(%0)"::"i"(n):"memory")
#define WAITV(n) asm volatile("s_waitcnt vmcnt(%0)"::"i"(n):"memory")
#define BAR() asm volatile("s_barrier":::"memory")
#define AUX_X 0
#define AUX_W 2
#define AUX_KV 2
#define AUX_O 0
#define ROT 1
constexpr int SLOT=32768;
constexpr int NS=4;
constexpr int LDS_BYTES=NS*SLOT+16384;
constexpr size_t O_BYTES=(size_t)2*512*4096*2;
constexpr size_t PART_OFF=O_BYTES;

__device__ __forceinline__ v4u cat(v2u a,v2u b){ return __builtin_shufflevector(a,b,0,1,2,3); }
__device__ __forceinline__ unsigned pk(float a,float b){ unsigned r; asm("v_cvt_pk_bf16_f32 %0, %1, %2":"=v"(r):"v"(a),"v"(b)); return r; }

struct Ptrs { const u16 *x0,*x1,*k0,*k1,*v0,*v1,*wq0,*wq1,*wo0,*wo1; u16* out; char* ws; };
#define M16Z(c,a,b) asm volatile("v_mfma_f32_16x16x32_bf16 %0, %1, %2, 0":"=v"(c):"v"(a),"v"(b))
#define FENCE4(n,a,b,c,d) asm volatile(n:"+v"(a),"+v"(b),"+v"(c),"+v"(d))

__device__ __forceinline__ void gemm_loop(f32x4 (&acc)[4][4], unsigned (&xc)[2], unsigned (&wc)[2][2]){
  v2u A[2][4][2]; v4u B[2][4];
  BAR();
  #pragma unroll
  for(int n=0;n<4;++n) RD128(B[0][n],xc[0],n*2048);
  #pragma unroll
  for(int m=0;m<4;++m){ RDTR(A[0][m][0],wc[m&1][0],(m>>1)*512); RDTR(A[0][m][1],wc[m&1][1],(m>>1)*512); }
  WAITL(0);
  #pragma unroll 1
  for(int s=0;s<16;++s){
    {
      int r=0;
      #pragma unroll
      for(int m=0;m<4;++m)
      #pragma unroll
      for(int n=0;n<4;++n){
        M16(acc[m][n],cat(A[0][m][0],A[0][m][1]),B[0][n]);
        if(r<4){ RD128(B[1][r],xc[1],r*2048); }
        else if(r<12){ int mm=(r-4)>>1,t=(r-4)&1; RDTR(A[1][mm][t],wc[mm&1][t],8192+(mm>>1)*512); }
        ++r;
      }
    }
    WAITL(0);
    BAR();
    {
      unsigned d = ((s&3)==3) ? (unsigned)(-3*SLOT) : (unsigned)SLOT;
      xc[0]+=d; xc[1]+=d; wc[0][0]+=d; wc[0][1]+=d; wc[1][0]+=d; wc[1][1]+=d;
    }
    {
      int r=0;
      #pragma unroll
      for(int m=0;m<4;++m)
      #pragma unroll
      for(int n=0;n<4;++n){
        M16(acc[m][n],cat(A[1][m][0],A[1][m][1]),B[1][n]);
        if(r<4){ RD128(B[0][r],xc[0],r*2048); }
        else if(r<12){ int mm=(r-4)>>1,t=(r-4)&1; RDTR(A[0][mm][t],wc[mm&1][t],(mm>>1)*512); }
        ++r;
      }
    }
    WAITL(0);
  }
}

__global__ __launch_bounds__(512) void k_p1(Ptrs P){
  extern __shared__ __attribute__((aligned(16))) char smem[];
  const int lane=threadIdx.x&63, w=__builtin_amdgcn_readfirstlane(threadIdx.x>>6);
  const int b=blockIdx.x, xcd=b&7, j=b>>3;
  const int br=xcd>>2, head=(xcd&3)*8+(j&7), mb=j>>3;
  if(w>=4){
    const int lw=w-4;
    const int rot= ROT==1 ? (4*mb+2*(j&7)) : 0;
    const u16* xb=(br?P.x1:P.x0)+(size_t)mb*128*1024;
    const u16* wb=(br?P.wq1:P.wq0)+head*128;
    __amdgpu_buffer_rsrc_t rx=__builtin_amdgcn_make_buffer_rsrc((void*)xb,0,128*2048,0x00020000);
    __amdgpu_buffer_rsrc_t rw=__builtin_amdgcn_make_buffer_rsrc((void*)wb,0,1024*8192-head*256,0x00020000);
    __amdgpu_buffer_rsrc_t rk=__builtin_amdgcn_make_buffer_rsrc((void*)((br?P.k1:P.k0)+(size_t)(mb*4*32+head)*4096),0,3*32*8192+8192,0x00020000);
    __amdgpu_buffer_rsrc_t rv=__builtin_amdgcn_make_buffer_rsrc((void*)((br?P.v1:P.v0)+(size_t)(mb*4*32+head)*4096),0,3*32*8192+8192,0x00020000);
    const int vx=(lane>>3)*2048 + 16*((lane&7) ^ (((lane>>4)&3)|((lw&1)<<2)));
    const int X=((((lane>>4)&1)^(lw&1))<<1);
    const int vw=((lane>>2)&7)*8192 + (lane>>5)*64 + 16*((lane&3)^X);
    const int vk=(lane>>4)*256 + 16*((lane&15) ^ (((lane>>4)<<2)|lw));
    LAS char* L=(LAS char*)smem;
    auto issue=[&](int s){
      int st=(s+rot)&15; int slot=(s&3)*SLOT; int k0=st*64;
      #pragma unroll
      for(int u=0;u<4;++u){ int i=lw+4*u; __builtin_amdgcn_raw_ptr_buffer_load_lds(rx,(LAS void*)(L+slot+i*1024),16,vx,i*8*2048+k0*2,0,AUX_X); }
      #pragma unroll
      for(int u=0;u<4;++u){ int rg=lw+4*(u>>1), chh=u&1; int i=rg*2+chh; __builtin_amdgcn_raw_ptr_buffer_load_lds(rw,(LAS void*)(L+slot+16384+i*1024),16,vw,(k0+rg*8)*8192+chh*128,0,AUX_W); }
    };
    auto issue_kv=[&](__amdgpu_buffer_rsrc_t r,int slot){
      #pragma unroll
      for(int u=0;u<8;++u){ int bb=u>>1, i=lw+4*(u&1); __builtin_amdgcn_raw_ptr_buffer_load_lds(r,(LAS void*)(L+slot*SLOT+bb*8192+i*1024),16,vk,bb*32*8192+i*1024,0,AUX_KV); }
    };
    issue(0); issue(1);
    WAITV(8); BAR();
    issue(2); issue(3);
    #pragma unroll 1
    for(int s=1;s<13;++s){ WAITV(16); BAR(); issue(s+3); }
    WAITV(16); BAR(); issue_kv(rk,0);
    WAITV(16); BAR(); issue_kv(rv,1);
    WAITV(16); BAR();
    WAITV(8); BAR();
    WAITV(0); BAR();
    return;
  }
  const unsigned lb=(unsigned)(size_t)(LAS char*)smem;
  const int g=lane>>4, i16=lane&15, q=i16>>2, p=lane&3;
  const int wm=w>>1, wn=w&1;
  unsigned xc[2], wc[2][2];
  { int row=wm*64+i16; int s=(row>>1)&7; xc[0]=lb+128*row+16*((g)^s); xc[1]=lb+128*row+16*((4+g)^s); }
  #pragma unroll
  for(int c1=0;c1<2;++c1)
  #pragma unroll
  for(int t=0;t<2;++t){ int X=((t^g)&1)<<1; wc[c1][t]=lb+16384+2048*g+64*(4*t+q)+16*((2*c1+(p>>1))^X)+8*(p&1)+1024*wn; }
  f32x4 acc[4][4];
  #pragma unroll
  for(int m=0;m<4;++m)
  #pragma unroll
  for(int n=0;n<4;++n) acc[m][n]=(f32x4){0,0,0,0};
  gemm_loop(acc,xc,wc);
  v2u Kf[2][2][2][2];
  {
    const int f=((i16&3)<<2)|((i16>>2)&3);
    #pragma unroll
    for(int mm=0;mm<2;++mm)
    #pragma unroll
    for(int t=0;t<2;++t){
      unsigned a=lb+wm*16384+256*i16+16*(((2*wn+mm)*4+2*t+(g>>1))^f)+8*(g&1);
      #pragma unroll
      for(int l=0;l<2;++l)
      #pragma unroll
      for(int kb=0;kb<2;++kb) RD64(Kf[l][kb][mm][t],a,l*8192+kb*4096);
    }
  }
  #pragma unroll
  for(int m=0;m<4;++m) FENCE4("s_nop 7",acc[m][0],acc[m][1],acc[m][2],acc[m][3]);
  v4u QB[4][2];
  #pragma unroll
  for(int n=0;n<4;++n)
  #pragma unroll
  for(int mm=0;mm<2;++mm){
    QB[n][mm]=(v4u){pk(acc[2*mm][n][0],acc[2*mm][n][1]),pk(acc[2*mm][n][2],acc[2*mm][n][3]),pk(acc[2*mm+1][n][0],acc[2*mm+1][n][1]),pk(acc[2*mm+1][n][2],acc[2*mm+1][n][3])};
  }
  WAITL(0);
  FENCE4("s_nop 1",QB[0][0],QB[0][1],QB[1][0],QB[1][1]);
  FENCE4("s_nop 1",QB[2][0],QB[2][1],QB[3][0],QB[3][1]);
  f32x4 S[2][2][2];
  #pragma unroll
  for(int l=0;l<2;++l)
  #pragma unroll
  for(int kb=0;kb<2;++kb)
  #pragma unroll
  for(int qb=0;qb<2;++qb) M16Z(S[l][kb][qb],cat(Kf[l][kb][0][0],Kf[l][kb][0][1]),QB[2*l+qb][0]);
  #pragma unroll
  for(int l=0;l<2;++l)
  #pragma unroll
  for(int kb=0;kb<2;++kb)
  #pragma unroll
  for(int qb=0;qb<2;++qb) M16(S[l][kb][qb],cat(Kf[l][kb][1][0],Kf[l][kb][1][1]),QB[2*l+qb][1]);
  FENCE4("s_nop 7",S[0][0][0],S[0][0][1],S[0][1][0],S[0][1][1]);
  FENCE4("s_nop 7",S[1][0][0],S[1][0][1],S[1][1][0],S[1][1][1]);
  {
    LAS char* sc=(LAS char*)smem+NS*SLOT+w*4096+lane*16;
    #pragma unroll
    for(int kb=0;kb<2;++kb)
    #pragma unroll
    for(int qb=0;qb<2;++qb) *(LAS f32x4*)(sc+(kb*2+qb)*1024) = wn? S[0][kb][qb] : S[1][kb][qb];
  }
  WAITL(0);
  asm volatile("":::"memory");
  BAR();
  f32x4 T[2][2];
  {
    LAS char* sc=(LAS char*)smem+NS*SLOT+(w^1)*4096+lane*16;
    #pragma unroll
    for(int kb=0;kb<2;++kb)
    #pragma unroll
    for(int qb=0;qb<2;++qb){ f32x4 o=*(LAS f32x4*)(sc+(kb*2+qb)*1024); T[kb][qb]=(wn? S[1][kb][qb] : S[0][kb][qb]) + o; }
  }
  v2u Vf[8][2];
  {
    const int qq=q;
    const int f=(qq<<2)|g;
    #pragma unroll
    for(int u=0;u<4;++u){
      unsigned a=lb+SLOT+(2*wm+wn)*8192+256*(4*g+qq)+16*((4*u+p)^f);
      #pragma unroll
      for(int hf=0;hf<2;++hf)
      #pragma unroll
      for(int t=0;t<2;++t) RDTR(Vf[2*u+hf][t],a,t*4096+hf*8);
    }
  }
  v4u PB[2];
  constexpr float SC=0.08838834764831845f*1.4426950408889634f;
  #pragma unroll
  for(int qb=0;qb<2;++qb){
    float mx=fmaxf(fmaxf(fmaxf(T[0][qb][0],T[0][qb][1]),fmaxf(T[0][qb][2],T[0][qb][3])),fmaxf(fmaxf(T[1][qb][0],T[1][qb][1]),fmaxf(T[1][qb][2],T[1][qb][3])));
    { auto r=__builtin_amdgcn_permlane16_swap(__float_as_uint(mx),__float_as_uint(mx),false,false); mx=fmaxf(__uint_as_float(r[0]),__uint_as_float(r[1])); }
    { auto r=__builtin_amdgcn_permlane32_swap(__float_as_uint(mx),__float_as_uint(mx),false,false); mx=fmaxf(__uint_as_float(r[0]),__uint_as_float(r[1])); }
    float e[8]; float sum=0;
    #pragma unroll
    for(int kb=0;kb<2;++kb)
    #pragma unroll
    for(int c=0;c<4;++c){ e[kb*4+c]=__builtin_amdgcn_exp2f((T[kb][qb][c]-mx)*SC); sum+=e[kb*4+c]; }
    { auto r=__builtin_amdgcn_permlane16_swap(__float_as_uint(sum),__float_as_uint(sum),false,false); sum=__uint_as_float(r[0])+__uint_as_float(r[1]); }
    { auto r=__builtin_amdgcn_permlane32_swap(__float_as_uint(sum),__float_as_uint(sum),false,false); sum=__uint_as_float(r[0])+__uint_as_float(r[1]); }
    float inv=__builtin_amdgcn_rcpf(sum);
    PB[qb]=(v4u){pk(e[0]*inv,e[1]*inv),pk(e[2]*inv,e[3]*inv),pk(e[4]*inv,e[5]*inv),pk(e[6]*inv,e[7]*inv)};
  }
  WAITL(0);
  asm volatile("s_nop 1":"+v"(PB[0]),"+v"(PB[1]));
  f32x4 O[8][2];
  #pragma unroll
  for(int m=0;m<8;++m)
  #pragma unroll
  for(int qb=0;qb<2;++qb) M16Z(O[m][qb],cat(Vf[m][0],Vf[m][1]),PB[qb]);
  #pragma unroll
  for(int m=0;m<8;m+=2) FENCE4("s_nop 7",O[m][0],O[m][1],O[m+1][0],O[m+1][1]);
  {
    char* ob=P.ws+(((size_t)(br*4+mb)*64+2*head)*16384)+(size_t)(2*(2*wm+wn))*2048+lane*16;
    #pragma unroll
    for(int mm=0;mm<4;++mm)
    #pragma unroll
    for(int qb=0;qb<2;++qb){
      v4u o=(v4u){pk(O[2*mm][qb][0],O[2*mm][qb][1]),pk(O[2*mm][qb][2],O[2*mm][qb][3]),pk(O[2*mm+1][qb][0],O[2*mm+1][qb][1]),pk(O[2*mm+1][qb][2],O[2*mm+1][qb][3])};
      *(GAS v4u*)(ob+(mm>>1)*16384+qb*2048+(mm&1)*1024)=o;
    }
  }
}

__global__ __launch_bounds__(512) void k_p2(Ptrs P){
  extern __shared__ __attribute__((aligned(16))) char smem[];
  const int lane=threadIdx.x&63, w=__builtin_amdgcn_readfirstlane(threadIdx.x>>6);
  const int b=blockIdx.x, xcd=b&7, j=b>>3;
  const int br=xcd>>2, kq=xcd&3, nb=j&7, mb=j>>3;
  if(w>=4){
    const int lw=w-4;
    const int rot= ROT==1 ? (4*mb+2*nb) : 0;
    const char* ob=P.ws+((size_t)(br*4+mb)*64+kq*16)*16384;
    const u16* wb=(br?P.wo1:P.wo0)+(size_t)kq*1024*1024+nb*128;
    __amdgpu_buffer_rsrc_t rx=__builtin_amdgcn_make_buffer_rsrc((void*)ob,0,16*16384,0x00020000);
    __amdgpu_buffer_rsrc_t rw=__builtin_amdgcn_make_buffer_rsrc((void*)wb,0,1024*2048-nb*256,0x00020000);
    const int vx=lane*16;
    const int X=((((lane>>4)&1)^(lw&1))<<1);
    const int vw=((lane>>2)&7)*2048 + (lane>>5)*64 + 16*((lane&3)^X);
    LAS char* L=(LAS char*)smem;
    auto issue=[&](int s){
      int st=(s+rot)&15; int slot=(s&3)*SLOT; int k0=st*64;
      #pragma unroll
      for(int u=0;u<4;++u){ int i=lw+4*u; __builtin_amdgcn_raw_ptr_buffer_load_lds(rx,(LAS void*)(L+slot+i*1024),16,vx,st*16384+i*1024,0,AUX_O); }
      #pragma unroll
      for(int u=0;u<4;++u){ int rg=lw+4*(u>>1), chh=u&1; int i=rg*2+chh; __builtin_amdgcn_raw_ptr_buffer_load_lds(rw,(LAS void*)(L+slot+16384+i*1024),16,vw,(k0+rg*8)*2048+chh*128,0,AUX_W); }
    };
    issue(0); issue(1);
    WAITV(8); BAR();
    issue(2); issue(3);
    #pragma unroll 1
    for(int s=1;s<13;++s){ WAITV(16); BAR(); issue(s+3); }
    WAITV(16); BAR();
    WAITV(8); BAR();
    WAITV(0); BAR();
    return;
  }
  const unsigned lb=(unsigned)(size_t)(LAS char*)smem;
  const int g=lane>>4, i16=lane&15, q=i16>>2, p=lane&3;
  const int wm=w>>1, wn=w&1;
  unsigned xc[2], wc[2][2];
  xc[0]=lb+lane*16+wm*8192; xc[1]=xc[0]+1024;
  #pragma unroll
  for(int c1=0;c1<2;++c1)
  #pragma unroll
  for(int t=0;t<2;++t){ int X=((t^g)&1)<<1; wc[c1][t]=lb+16384+2048*g+64*(4*t+q)+16*((2*c1+(p>>1))^X)+8*(p&1)+1024*wn; }
  f32x4 acc[4][4];
  #pragma unroll
  for(int m=0;m<4;++m)
  #pragma unroll
  for(int n=0;n<4;++n) acc[m][n]=(f32x4){0,0,0,0};
  gemm_loop(acc,xc,wc);
  #pragma unroll
  for(int m=0;m<4;++m) FENCE4("s_nop 7",acc[m][0],acc[m][1],acc[m][2],acc[m][3]);
  char* pb=P.ws+PART_OFF+((((size_t)(br*4+mb)*8+nb)*4+kq)*4+w)*16384+lane*16;
  #pragma unroll
  for(int m=0;m<4;++m)
  #pragma unroll
  for(int n=0;n<4;++n) *(GAS f32x4*)(pb+(m*4+n)*1024)=acc[m][n];
}

__global__ __launch_bounds__(256) void k_p3(Ptrs P){
  const int lane=threadIdx.x&63, wv=threadIdx.x>>6;
  const int id=blockIdx.x*4+wv;
  const int n=id&3, w=(id>>2)&3, tg=id>>4;
  const int nb=tg&7, mb=(tg>>3)&3, br=tg>>5;
  const int g=lane>>4,i16=lane&15;
  const char* pb=P.ws+PART_OFF+((size_t)tg*4*4+w)*16384+lane*16;
  const int wm=w>>1, wn=w&1;
  const int row=mb*128+wm*64+16*n+i16;
  #pragma unroll
  for(int m=0;m<4;++m){
    f32x4 s=*(const GAS f32x4*)(pb+(m*4+n)*1024);
    #pragma unroll
    for(int kq=1;kq<4;++kq) s+=*(const GAS f32x4*)(pb+(size_t)kq*65536+(m*4+n)*1024);
    v2u o=(v2u){pk(s[0],s[1]),pk(s[2],s[3])};
    *(GAS v2u*)(P.out+(size_t)row*2048+br*1024+nb*128+wn*64+16*m+4*g)=o;
  }
}

extern "C" void kernel_launch(void* const* d_in, const int* in_sizes, int n_in,
                              void* d_out, int out_size, void* d_ws, size_t ws_size,
                              hipStream_t stream) {
  Ptrs P;
  P.x0=(const u16*)d_in[0]; P.k0=(const u16*)d_in[1]; P.v0=(const u16*)d_in[2]; P.wq0=(const u16*)d_in[3]; P.wo0=(const u16*)d_in[4];
  P.x1=(const u16*)d_in[5]; P.k1=(const u16*)d_in[6]; P.v1=(const u16*)d_in[7]; P.wq1=(const u16*)d_in[8]; P.wo1=(const u16*)d_in[9];
  P.out=(u16*)d_out; P.ws=(char*)d_ws;
  (void)hipFuncSetAttribute((const void*)k_p1,hipFuncAttributeMaxDynamicSharedMemorySize,LDS_BYTES);
  (void)hipFuncSetAttribute((const void*)k_p2,hipFuncAttributeMaxDynamicSharedMemorySize,LDS_BYTES);
  k_p1<<<256,512,LDS_BYTES,stream>>>(P);
  k_p2<<<256,512,LDS_BYTES,stream>>>(P);
  k_p3<<<256,256,0,stream>>>(P);
}
```

```cpp
#include <hip/hip_runtime.h>
#include <hip/hip_bf16.h>
typedef unsigned v4u __attribute__((ext_vector_type(4)));
typedef unsigned v2u __attribute__((ext_vector_type(2)));
typedef float f32x4 __attribute__((ext_vector_type(4)));
typedef unsigned short u16;
#define GAS __attribute__((address_space(1)))
#define LAS __attribute__((address_space(3)))
#define M16(c,a,b) asm volatile("v_mfma_f32_16x16x32_bf16 %0, %1, %2, %0":"+v"(c):"v"(a),"v"(b))
#define M16Z(c,a,b) asm volatile("v_mfma_f32_16x16x32_bf16 %0, %1, %2, 0":"=v"(c):"v"(a),"v"(b))
#define FENCE4(n,a,b,c,d) asm volatile(n:"+v"(a),"+v"(b),"+v"(c),"+v"(d))
#define RD128(d,addr,off) asm volatile("ds_read_b128 %0, %1 offset:%2":"=v"(d):"v"(addr),"i"(off))
#define RD64(d,addr,off) asm volatile("ds_read_b64 %0, %1 offset:%2":"=v"(d):"v"(addr),"i"(off))
#define RDTR(d,addr,off) asm volatile("ds_read_b64_tr_b16 %0, %1 offset:%2":"=v"(d):"v"(addr),"i"(off))
#define WR128(addr,off,d) asm volatile("ds_write_b128 %0, %1 offset:%2"::"v"(addr),"v"(d),"i"(off):"memory")
#define WAITL(n) asm volatile("s_waitcnt lgkmcnt(%0)"::"i"(n):"memory")
#define WAITV(n) asm volatile("s_waitcnt vmcnt(%0)"::"i"(n):"memory")
#define BAR() asm volatile("s_barrier":::"memory")
#define RLX_AGENT __ATOMIC_RELAXED, __HIP_MEMORY_SCOPE_AGENT
#define AUX_KV 2
#define PART_BF16 1
constexpr int SC1=16;
constexpr int SLOT=32768;
constexpr int SCR=4*SLOT;
constexpr int LDS_BYTES=SCR+16384;
constexpr size_t O_OFF=0;
constexpr size_t PART_OFF=(size_t)8<<20;
constexpr size_t FLAG_OFF=(size_t)32<<20;
constexpr int OF_W=0;
constexpr int RF_W=32*32;
constexpr int FLAG_BYTES=(32*32+64*32)*4;
constexpr unsigned SPIN_CAP=1u<<16;

#define CST(i)
#define LST(i)

__device__ __forceinline__ v4u cat(v2u a,v2u b){ return __builtin_shufflevector(a,b,0,1,2,3); }
__device__ __forceinline__ unsigned pk(float a,float b){ unsigned r; asm("v_cvt_pk_bf16_f32 %0, %1, %2":"=v"(r):"v"(a),"v"(b)); return r; }
__device__ __forceinline__ float bflo(unsigned u){ return __uint_as_float(u<<16); }
__device__ __forceinline__ float bfhi(unsigned u){ return __uint_as_float(u&0xffff0000u); }

struct Ptrs { const u16 *x0,*x1,*k0,*k1,*v0,*v1,*wq0,*wq1,*wo0,*wo1; u16* out; char* ws; };

template<bool PUB>
__device__ __forceinline__ void gemm_loop(f32x4 (&acc)[4][4], unsigned (&xc)[2], unsigned (&wc)[2][2], GAS unsigned* flag, int lane){
  v2u A[2][4][2]; v4u B[2][4];
  #pragma unroll
  for(int n=0;n<4;++n) RD128(B[0][n],xc[0],n*2048);
  #pragma unroll
  for(int m=0;m<4;++m){ RDTR(A[0][m][0],wc[m&1][0],(m>>1)*512); RDTR(A[0][m][1],wc[m&1][1],(m>>1)*512); }
  WAITL(0);
  #pragma unroll 1
  for(int s=0;s<16;++s){
    {
      int r=0;
      #pragma unroll
      for(int m=0;m<4;++m)
      #pragma unroll
      for(int n=0;n<4;++n){
        M16(acc[m][n],cat(A[0][m][0],A[0][m][1]),B[0][n]);
        if(r<4){ RD128(B[1][r],xc[1],r*2048); }
        else if(r<12){ int mm=(r-4)>>1,t=(r-4)&1; RDTR(A[1][mm][t],wc[mm&1][t],8192+(mm>>1)*512); }
        ++r;
      }
    }
    if(PUB){ if(s==0){ WAITV(0); if(lane==0) __hip_atomic_store(flag,1u,RLX_AGENT); } }
    WAITL(0);
    BAR();
    {
      unsigned d = ((s&3)==3) ? (unsigned)(-3*SLOT) : (unsigned)SLOT;
      xc[0]+=d; xc[1]+=d; wc[0][0]+=d; wc[0][1]+=d; wc[1][0]+=d; wc[1][1]+=d;
    }
    {
      int r=0;
      #pragma unroll
      for(int m=0;m<4;++m)
      #pragma unroll
      for(int n=0;n<4;++n){
        M16(acc[m][n],cat(A[1][m][0],A[1][m][1]),B[1][n]);
        if(r<4){ RD128(B[0][r],xc[0],r*2048); }
        else if(r<12){ int mm=(r-4)>>1,t=(r-4)&1; RDTR(A[0][mm][t],wc[mm&1][t],(mm>>1)*512); }
        ++r;
      }
    }
    WAITL(0);
  }
}

__device__ __forceinline__ bool poll_all(GAS unsigned* p, bool active){
  for(unsigned spins=0;spins<SPIN_CAP;++spins){
    unsigned v = active ? __hip_atomic_load(p,RLX_AGENT) : 1u;
    if(__all(v!=0u)) return true;
    __builtin_amdgcn_s_sleep(1);
  }
  return false;
}

__global__ __launch_bounds__(512) void k_fused(Ptrs P){
  extern __shared__ __attribute__((aligned(16))) char smem[];
  const int lane=threadIdx.x&63, w=__builtin_amdgcn_readfirstlane(threadIdx.x>>6);
  const int b=blockIdx.x, g8=b&7, j=b>>3;
  const int br=g8>>2, oct=g8&3, jj=j&7, mb=j>>3;
  const int head=oct*8+jj;
  GAS unsigned* flags=(GAS unsigned*)(P.ws+FLAG_OFF);
  const int ogrp=(br*4+mb)*4+oct;
  const int rgrp=(br*4+mb)*8+jj;
  if(w>=4){
    const int lw=w-4;
    LAS char* L=(LAS char*)smem;
    const unsigned lb=(unsigned)(size_t)L;
    LST(0);
    const int rot=(4*mb+2*jj);
    const __amdgpu_buffer_rsrc_t rx=__builtin_amdgcn_make_buffer_rsrc((void*)((br?P.x1:P.x0)+(size_t)mb*128*1024),0,128*2048,0x00020000);
    const __amdgpu_buffer_rsrc_t rw=__builtin_amdgcn_make_buffer_rsrc((void*)((br?P.wq1:P.wq0)+head*128),0,1024*8192-head*256,0x00020000);
    const int vx=(lane>>3)*2048 + 16*((lane&7) ^ (((lane>>4)&3)|((lw&1)<<2)));
    const int X=((((lane>>4)&1)^(lw&1))<<1);
    const int vw=((lane>>2)&7)*8192 + (lane>>5)*64 + 16*((lane&3)^X);
    auto issue1=[&](int s){
      int st=(s+rot)&15; int slot=(s&3)*SLOT; int k0=st*64;
      #pragma unroll
      for(int u=0;u<4;++u){ int i=lw+4*u; __builtin_amdgcn_raw_ptr_buffer_load_lds(rx,(LAS void*)(L+slot+i*1024),16,vx,i*8*2048+k0*2,0,0); }
      #pragma unroll
      for(int u=0;u<4;++u){ int rg=lw+4*(u>>1), chh=u&1; int i=rg*2+chh; __builtin_amdgcn_raw_ptr_buffer_load_lds(rw,(LAS void*)(L+slot+16384+i*1024),16,vw,(k0+rg*8)*8192+chh*128,0,0); }
    };
    issue1(0); issue1(1);
    WAITV(8); BAR(); LST(1);
    issue1(2); issue1(3);
    const __amdgpu_buffer_rsrc_t rk=__builtin_amdgcn_make_buffer_rsrc((void*)((br?P.k1:P.k0)+(size_t)(mb*4*32+head)*4096),0,3*32*8192+8192,0x00020000);
    const __amdgpu_buffer_rsrc_t rv=__builtin_amdgcn_make_buffer_rsrc((void*)((br?P.v1:P.v0)+(size_t)(mb*4*32+head)*4096),0,3*32*8192+8192,0x00020000);
    const int vk=(lane>>4)*256 + 16*((lane&15) ^ (((lane>>4)<<2)|lw));
    v4u Kr[8], Vr[8];
    #pragma unroll 1
    for(int s=1;s<6;++s){ WAITV(16); BAR(); LST(1+s); issue1(s+3); }
    #pragma unroll
    for(int u=0;u<8;++u){ int so=(u>>1)*32*8192+(lw+4*(u&1))*1024; asm volatile("buffer_load_dwordx4 %0, %1, %2, %3 offen nt":"=v"(Kr[u]):"v"(vk),"s"(rk),"s"(so):"memory"); }
    #pragma unroll 1
    for(int s=6;s<9;++s){ WAITV(16); BAR(); LST(1+s); issue1(s+3); }
    #pragma unroll
    for(int u=0;u<8;++u){ int so=(u>>1)*32*8192+(lw+4*(u&1))*1024; asm volatile("buffer_load_dwordx4 %0, %1, %2, %3 offen nt":"=v"(Vr[u]):"v"(vk),"s"(rv),"s"(so):"memory"); }
    #pragma unroll 1
    for(int s=9;s<13;++s){ WAITV(16); BAR(); LST(1+s); issue1(s+3); }
    const __amdgpu_buffer_rsrc_t rwo=__builtin_amdgcn_make_buffer_rsrc((void*)((br?P.wo1:P.wo0)+(size_t)oct*1024*1024+jj*128),0,1024*2048-jj*256,0x00020000);
    const __amdgpu_buffer_rsrc_t ro=__builtin_amdgcn_make_buffer_rsrc((void*)(P.ws+O_OFF+((size_t)(br*4+mb)*64+oct*16)*16384),0,16*16384,0x00020000);
    const int vw2=((lane>>2)&7)*2048 + (lane>>5)*64 + 16*((lane&3)^X);
    auto st2=[&](int r)->int{
      if(r<2) return 2*jj+r;
      int t=((r-2)>>1)+2*mb; if(t>=7) t-=7; if(t>=7) t-=7;
      return 2*((jj+1+t)&7)+(r&1);
    };
    auto issue_wo=[&](int r){
      int slot=(r&3)*SLOT; int k0=st2(r)*64;
      #pragma unroll
      for(int u=0;u<4;++u){ int rg=lw+4*(u>>1), chh=u&1; int i=rg*2+chh; __builtin_amdgcn_raw_ptr_buffer_load_lds(rwo,(LAS void*)(L+slot+16384+i*1024),16,vw2,(k0+rg*8)*2048+chh*128,0,0); }
    };
    auto issue_o=[&](int r){
      int slot=(r&3)*SLOT; int so=st2(r)*16384;
      #pragma unroll
      for(int u=0;u<4;++u){ int i=lw+4*u; __builtin_amdgcn_raw_ptr_buffer_load_lds(ro,(LAS void*)(L+slot+i*1024),16,lane*16,so+i*1024,0,SC1); }
    };
    const unsigned kva=lb+lw*1024+lane*16;
    WAITV(16); BAR(); LST(14);
    #pragma unroll
    for(int u=0;u<8;++u){ asm volatile("":"+v"(Kr[u])); asm volatile("":"+v"(Vr[u])); }
    #pragma unroll
    for(int u=0;u<4;++u) WR128(kva,(u>>1)*8192+(u&1)*4096,Kr[u]);
    issue_wo(0);
    WAITV(12); BAR(); LST(15);
    #pragma unroll
    for(int u=4;u<8;++u) WR128(kva,SLOT+((u-4)>>1)*8192+(u&1)*4096,Kr[u]);
    issue_wo(1);
    WAITV(8); WAITL(0); BAR(); LST(16);
    #pragma unroll
    for(int u=0;u<4;++u) asm volatile("ds_write_b128 %0, %1 offset:%2"::"v"(kva+2*SLOT),"v"(Vr[u]),"i"((u>>1)*8192+(u&1)*4096):"memory");
    issue_wo(2);
    WAITL(0); BAR(); LST(17);
    #pragma unroll
    for(int u=4;u<8;++u) asm volatile("ds_write_b128 %0, %1 offset:%2"::"v"(kva+3*SLOT),"v"(Vr[u]),"i"(((u-4)>>1)*8192+(u&1)*4096):"memory");
    issue_wo(3);
    WAITL(0); BAR(); LST(18);
    WAITV(12); BAR(); LST(19);
    WAITV(8); BAR(); LST(20);
    issue_wo(4);
    poll_all(flags+OF_W+ogrp*32+(lane&31),(lane<32)&&((lane>>2)!=jj));
    LST(21);
    issue_o(2); issue_o(3); issue_o(4);
    WAITV(8); BAR(); LST(22);
    issue_o(5); issue_wo(5);
    WAITV(12); BAR(); LST(23);
    #pragma unroll 1
    for(int r=4;r<14;++r){ issue_o(r+2); issue_wo(r+2); WAITV(16); BAR(); }
    LST(24);
    WAITV(8); BAR();
    WAITV(0); BAR();
    LST(25);
    return;
  }
  const unsigned lb=(unsigned)(size_t)(LAS char*)smem;
  const int g=lane>>4, i16=lane&15, q=i16>>2, p=lane&3;
  const int wm=w>>1, wn=w&1;
  unsigned xc[2], wc[2][2];
  { int row=wm*64+i16; int s=(row>>1)&7; xc[0]=lb+128*row+16*((g)^s); xc[1]=lb+128*row+16*((4+g)^s); }
  #pragma unroll
  for(int c1=0;c1<2;++c1)
  #pragma unroll
  for(int t=0;t<2;++t){ int X=((t^g)&1)<<1; wc[c1][t]=lb+16384+2048*g+64*(4*t+q)+16*((2*c1+(p>>1))^X)+8*(p&1)+1024*wn; }
  f32x4 acc[4][4];
  #pragma unroll
  for(int m=0;m<4;++m)
  #pragma unroll
  for(int n=0;n<4;++n) acc[m][n]=(f32x4){0,0,0,0};
  BAR();
  CST(30);
  gemm_loop<false>(acc,xc,wc,nullptr,lane);
  CST(31);
  v2u Kf[2][2][2][2];
  {
    const int f=((i16&3)<<2)|((i16>>2)&3);
    #pragma unroll
    for(int mm=0;mm<2;++mm)
    #pragma unroll
    for(int t=0;t<2;++t){
      unsigned a=lb+wm*SLOT+256*i16+16*(((2*wn+mm)*4+2*t+(g>>1))^f)+8*(g&1);
      #pragma unroll
      for(int l=0;l<2;++l)
      #pragma unroll
      for(int kb=0;kb<2;++kb) RD64(Kf[l][kb][mm][t],a,l*8192+kb*4096);
    }
  }
  #pragma unroll
  for(int m=0;m<4;++m) FENCE4("s_nop 1",acc[m][0],acc[m][1],acc[m][2],acc[m][3]);
  v4u QB[4][2];
  #pragma unroll
  for(int n=0;n<4;++n)
  #pragma unroll
  for(int mm=0;mm<2;++mm){
    QB[n][mm]=(v4u){pk(acc[2*mm][n][0],acc[2*mm][n][1]),pk(acc[2*mm][n][2],acc[2*mm][n][3]),pk(acc[2*mm+1][n][0],acc[2*mm+1][n][1]),pk(acc[2*mm+1][n][2],acc[2*mm+1][n][3])};
  }
  WAITL(0);
  FENCE4("s_nop 1",QB[0][0],QB[0][1],QB[1][0],QB[1][1]);
  FENCE4("s_nop 1",QB[2][0],QB[2][1],QB[3][0],QB[3][1]);
  f32x4 S[2][2][2];
  #pragma unroll
  for(int l=0;l<2;++l)
  #pragma unroll
  for(int kb=0;kb<2;++kb)
  #pragma unroll
  for(int qb=0;qb<2;++qb) M16Z(S[l][kb][qb],cat(Kf[l][kb][0][0],Kf[l][kb][0][1]),QB[2*l+qb][0]);
  #pragma unroll
  for(int l=0;l<2;++l)
  #pragma unroll
  for(int kb=0;kb<2;++kb)
  #pragma unroll
  for(int qb=0;qb<2;++qb) M16(S[l][kb][qb],cat(Kf[l][kb][1][0],Kf[l][kb][1][1]),QB[2*l+qb][1]);
  FENCE4("s_nop 7",S[0][0][0],S[0][0][1],S[0][1][0],S[0][1][1]);
  FENCE4("s_nop 0",S[1][0][0],S[1][0][1],S[1][1][0],S[1][1][1]);
  {
    LAS char* sc=(LAS char*)smem+SCR+w*4096+lane*16;
    #pragma unroll
    for(int kb=0;kb<2;++kb)
    #pragma unroll
    for(int qb=0;qb<2;++qb) *(LAS f32x4*)(sc+(kb*2+qb)*1024) = wn? S[0][kb][qb] : S[1][kb][qb];
  }
  WAITL(0);
  asm volatile("":::"memory");
  BAR();
  CST(32);
  f32x4 T[2][2];
  {
    LAS char* sc=(LAS char*)smem+SCR+(w^1)*4096+lane*16;
    #pragma unroll
    for(int kb=0;kb<2;++kb)
    #pragma unroll
    for(int qb=0;qb<2;++qb){ f32x4 o=*(LAS f32x4*)(sc+(kb*2+qb)*1024); T[kb][qb]=(wn? S[1][kb][qb] : S[0][kb][qb]) + o; }
  }
  v2u Vf[8][2];
  {
    const int f=(q<<2)|g;
    #pragma unroll
    for(int u=0;u<4;++u){
      unsigned a=lb+(2+wm)*SLOT+wn*8192+256*(4*g+q)+16*((4*u+p)^f);
      #pragma unroll
      for(int hf=0;hf<2;++hf)
      #pragma unroll
      for(int t=0;t<2;++t) RDTR(Vf[2*u+hf][t],a,t*4096+hf*8);
    }
  }
  v4u PB[2];
  constexpr float SCL=0.08838834764831845f*1.4426950408889634f;
  #pragma unroll
  for(int qb=0;qb<2;++qb){
    float mx=fmaxf(fmaxf(fmaxf(T[0][qb][0],T[0][qb][1]),fmaxf(T[0][qb][2],T[0][qb][3])),fmaxf(fmaxf(T[1][qb][0],T[1][qb][1]),fmaxf(T[1][qb][2],T[1][qb][3])));
    { auto r=__builtin_amdgcn_permlane16_swap(__float_as_uint(mx),__float_as_uint(mx),false,false); mx=fmaxf(__uint_as_float(r[0]),__uint_as_float(r[1])); }
    { auto r=__builtin_amdgcn_permlane32_swap(__float_as_uint(mx),__float_as_uint(mx),false,false); mx=fmaxf(__uint_as_float(r[0]),__uint_as_float(r[1])); }
    float e[8]; float sum=0; const float nm=-mx*SCL;
    #pragma unroll
    for(int kb=0;kb<2;++kb)
    #pragma unroll
    for(int c=0;c<4;++c){ e[kb*4+c]=__builtin_amdgcn_exp2f(__builtin_fmaf(T[kb][qb][c],SCL,nm)); sum+=e[kb*4+c]; }
    { auto r=__builtin_amdgcn_permlane16_swap(__float_as_uint(sum),__float_as_uint(sum),false,false); sum=__uint_as_float(r[0])+__uint_as_float(r[1]); }
    { auto r=__builtin_amdgcn_permlane32_swap(__float_as_uint(sum),__float_as_uint(sum),false,false); sum=__uint_as_float(r[0])+__uint_as_float(r[1]); }
    float inv=__builtin_amdgcn_rcpf(sum);
    PB[qb]=(v4u){pk(e[0]*inv,e[1]*inv),pk(e[2]*inv,e[3]*inv),pk(e[4]*inv,e[5]*inv),pk(e[6]*inv,e[7]*inv)};
  }
  WAITL(0);
  asm volatile("s_nop 1":"+v"(PB[0]),"+v"(PB[1]));
  f32x4 O[8][2];
  #pragma unroll
  for(int m=0;m<8;++m)
  #pragma unroll
  for(int qb=0;qb<2;++qb) M16Z(O[m][qb],cat(Vf[m][0],Vf[m][1]),PB[qb]);
  FENCE4("s_nop 7",O[6][0],O[6][1],O[7][0],O[7][1]);
  #pragma unroll
  for(int m=0;m<6;m+=2) FENCE4("",O[m][0],O[m][1],O[m+1][0],O[m+1][1]);
  {
    const __amdgpu_buffer_rsrc_t rs=__builtin_amdgcn_make_buffer_rsrc((void*)(P.ws+O_OFF+((size_t)(br*4+mb)*64+2*head)*16384),0,2*16384,0x00020000);
    const int bb=2*wm+wn;
    const unsigned oa=lb+bb*4096+lane*16;
    #pragma unroll
    for(int mm=0;mm<4;++mm)
    #pragma unroll
    for(int qb=0;qb<2;++qb){
      v4u o=(v4u){pk(O[2*mm][qb][0],O[2*mm][qb][1]),pk(O[2*mm][qb][2],O[2*mm][qb][3]),pk(O[2*mm+1][qb][0],O[2*mm+1][qb][1]),pk(O[2*mm+1][qb][2],O[2*mm+1][qb][3])};
      __builtin_amdgcn_raw_buffer_store_b128(o,rs,bb*4096+lane*16,(mm>>1)*16384+qb*2048+(mm&1)*1024,SC1);
      WR128(oa,(mm>>1)*SLOT+qb*2048+(mm&1)*1024,o);
    }
  }
  xc[0]=lb+lane*16+wm*8192; xc[1]=xc[0]+1024;
  #pragma unroll
  for(int c1=0;c1<2;++c1)
  #pragma unroll
  for(int t=0;t<2;++t){ int X=((t^g)&1)<<1; wc[c1][t]=lb+16384+2048*g+64*(4*t+q)+16*((2*c1+(p>>1))^X)+8*(p&1)+1024*wn; }
  #pragma unroll
  for(int m=0;m<4;++m)
  #pragma unroll
  for(int n=0;n<4;++n) acc[m][n]=(f32x4){0,0,0,0};
  WAITL(0);
  CST(33);
  BAR();
  CST(34);
  gemm_loop<true>(acc,xc,wc,flags+OF_W+ogrp*32+jj*4+w,lane);
  CST(35);
  #pragma unroll
  for(int m=0;m<4;++m) FENCE4("s_nop 1",acc[m][0],acc[m][1],acc[m][2],acc[m][3]);
  {
    constexpr int TB = PART_BF16? 512:1024;
    const __amdgpu_buffer_rsrc_t rp=__builtin_amdgcn_make_buffer_rsrc((void*)(P.ws+PART_OFF+(size_t)rgrp*(4*4*4*4*TB)),0,4*4*4*4*TB,0x00020000);
    #pragma unroll
    for(int n=0;n<4;++n){
      if(n!=oct){
        #pragma unroll
        for(int m=0;m<4;m+=(PART_BF16?2:1)){
          if(PART_BF16){
            v4u o=(v4u){pk(acc[m][n][0],acc[m][n][1]),pk(acc[m][n][2],acc[m][n][3]),pk(acc[m+1][n][0],acc[m+1][n][1]),pk(acc[m+1][n][2],acc[m+1][n][3])};
            __builtin_amdgcn_raw_buffer_store_b128(o,rp,lane*16,(((n*4+oct)*4+w)*4+m)*TB,SC1);
          } else {
            __builtin_amdgcn_raw_buffer_store_b128(__builtin_bit_cast(v4u,acc[m][n]),rp,lane*16,(((n*4+oct)*4+w)*4+m)*TB,SC1);
          }
        }
      }
    }
    WAITV(0);
    if(lane==0) __hip_atomic_store(flags+RF_W+rgrp*32+oct*4+w,1u,RLX_AGENT);
    CST(36);
    f32x4 mine[4];
    #pragma unroll
    for(int m=0;m<4;++m) mine[m]= oct==0?acc[m][0]: oct==1?acc[m][1]: oct==2?acc[m][2]:acc[m][3];
    poll_all(flags+RF_W+rgrp*32+(lane&3)*4+w,(lane<4)&&(lane!=oct));
    CST(37);
    v4u in[3][PART_BF16?2:4];
    #pragma unroll
    for(int e=0;e<3;++e){
      int src=e+(e>=oct?1:0);
      #pragma unroll
      for(int m=0;m<(PART_BF16?2:4);++m) in[e][m]=__builtin_amdgcn_raw_buffer_load_b128(rp,lane*16,(((oct*4+src)*4+w)*4+(PART_BF16?2*m:m))*TB,SC1);
    }
    #pragma unroll
    for(int e=0;e<3;++e){
      if(PART_BF16){
        #pragma unroll
        for(int m=0;m<2;++m){
          mine[2*m][0]+=bflo(in[e][m][0]); mine[2*m][1]+=bfhi(in[e][m][0]); mine[2*m][2]+=bflo(in[e][m][1]); mine[2*m][3]+=bfhi(in[e][m][1]);
          mine[2*m+1][0]+=bflo(in[e][m][2]); mine[2*m+1][1]+=bfhi(in[e][m][2]); mine[2*m+1][2]+=bflo(in[e][m][3]); mine[2*m+1][3]+=bfhi(in[e][m][3]);
        }
      } else {
        #pragma unroll
        for(int m=0;m<4;++m) mine[m]+=__builtin_bit_cast(f32x4,in[e][m]);
      }
    }
    u16* op=P.out+(size_t)(mb*128+wm*64+16*oct+i16)*2048+br*1024+jj*128+wn*64+4*g;
    #pragma unroll
    for(int m=0;m<4;++m) *(GAS v2u*)(op+16*m)=(v2u){pk(mine[m][0],mine[m][1]),pk(mine[m][2],mine[m][3])};
    CST(38);
  }
}

extern "C" void kernel_launch(void* const* d_in, const int* in_sizes, int n_in,
                              void* d_out, int out_size, void* d_ws, size_t ws_size,
                              hipStream_t stream) {
  Ptrs P;
  P.x0=(const u16*)d_in[0]; P.k0=(const u16*)d_in[1]; P.v0=(const u16*)d_in[2]; P.wq0=(const u16*)d_in[3]; P.wo0=(const u16*)d_in[4];
  P.x1=(const u16*)d_in[5]; P.k1=(const u16*)d_in[6]; P.v1=(const u16*)d_in[7]; P.wq1=(const u16*)d_in[8]; P.wo1=(const u16*)d_in[9];
  P.out=(u16*)d_out; P.ws=(char*)d_ws;
  const int lds=LDS_BYTES;
  (void)hipFuncSetAttribute((const void*)k_fused,hipFuncAttributeMaxDynamicSharedMemorySize,lds);
  (void)hipMemsetAsync((char*)d_ws+FLAG_OFF,0,FLAG_BYTES,stream);
  k_fused<<<256,512,lds,stream>>>(P);
}
```

```cpp
#define TIMELINE 1
#define NO_DUMP 1
#include <hip/hip_runtime.h>
#include <hip/hip_bf16.h>
typedef unsigned v4u __attribute__((ext_vector_type(4)));
typedef unsigned v2u __attribute__((ext_vector_type(2)));
typedef float f32x4 __attribute__((ext_vector_type(4)));
typedef unsigned short u16;
#define GAS __attribute__((address_space(1)))
#define LAS __attribute__((address_space(3)))
#define M16(c,a,b) asm volatile("v_mfma_f32_16x16x32_bf16 %0, %1, %2, %0":"+v"(c):"v"(a),"v"(b))
#define M16Z(c,a,b) asm volatile("v_mfma_f32_16x16x32_bf16 %0, %1, %2, 0":"=v"(c):"v"(a),"v"(b))
#define FENCE4(n,a,b,c,d) asm volatile(n:"+v"(a),"+v"(b),"+v"(c),"+v"(d))
#define RD128(d,addr,off) asm volatile("ds_read_b128 %0, %1 offset:%2":"=v"(d):"v"(addr),"i"(off))
#define RD64(d,addr,off) asm volatile("ds_read_b64 %0, %1 offset:%2":"=v"(d):"v"(addr),"i"(off))
#define RDTR(d,addr,off) asm volatile("ds_read_b64_tr_b16 %0, %1 offset:%2":"=v"(d):"v"(addr),"i"(off))
#define WR128(addr,off,d) asm volatile("ds_write_b128 %0, %1 offset:%2"::"v"(addr),"v"(d),"i"(off):"memory")
#define WAITL(n) asm volatile("s_waitcnt lgkmcnt(%0)"::"i"(n):"memory")
#define WAITV(n) asm volatile("s_waitcnt vmcnt(%0)"::"i"(n):"memory")
#define BAR() asm volatile("s_barrier":::"memory")
#define RLX_AGENT __ATOMIC_RELAXED, __HIP_MEMORY_SCOPE_AGENT
#define AUX_KV 2
#define PART_BF16 1
constexpr int SC1=16;
constexpr int SLOT=32768;
constexpr int SCR=4*SLOT;
constexpr int LDS_BYTES=SCR+16384;
constexpr size_t O_OFF=0;
constexpr size_t PART_OFF=(size_t)8<<20;
constexpr size_t FLAG_OFF=(size_t)32<<20;
constexpr int OF_W=0;
constexpr int RF_W=32*32;
constexpr int FLAG_BYTES=(32*32+64*32)*4;
constexpr unsigned SPIN_CAP=1u<<16;

#define STAMP(idx) do{ unsigned long long t_; asm volatile("s_memrealtime %0\n\ts_waitcnt lgkmcnt(0)":"=s"(t_)::"memory"); if(lane==0) ((volatile LAS unsigned*)((LAS char*)smem+LDS_BYTES))[(idx)]=(unsigned)t_; }while(0)
#define CST(idx) do{ if(w==0) STAMP(idx); }while(0)
#define LST(idx) do{ if(w==4) STAMP(idx); }while(0)

__device__ __forceinline__ v4u cat(v2u a,v2u b){ return __builtin_shufflevector(a,b,0,1,2,3); }
__device__ __forceinline__ unsigned pk(float a,float b){ unsigned r; asm("v_cvt_pk_bf16_f32 %0, %1, %2":"=v"(r):"v"(a),"v"(b)); return r; }
__device__ __forceinline__ float bflo(unsigned u){ return __uint_as_float(u<<16); }
__device__ __forceinline__ float bfhi(unsigned u){ return __uint_as_float(u&0xffff0000u); }

struct Ptrs { const u16 *x0,*x1,*k0,*k1,*v0,*v1,*wq0,*wq1,*wo0,*wo1; u16* out; char* ws; };

template<bool PUB>
__device__ __forceinline__ void gemm_loop(f32x4 (&acc)[4][4], unsigned (&xc)[2], unsigned (&wc)[2][2], GAS unsigned* flag, int lane){
  v2u A[2][4][2]; v4u B[2][4];
  #pragma unroll
  for(int n=0;n<4;++n) RD128(B[0][n],xc[0],n*2048);
  #pragma unroll
  for(int m=0;m<4;++m){ RDTR(A[0][m][0],wc[m&1][0],(m>>1)*512); RDTR(A[0][m][1],wc[m&1][1],(m>>1)*512); }
  WAITL(0);
  #pragma unroll 1
  for(int s=0;s<16;++s){
    {
      int r=0;
      #pragma unroll
      for(int m=0;m<4;++m)
      #pragma unroll
      for(int n=0;n<4;++n){
        M16(acc[m][n],cat(A[0][m][0],A[0][m][1]),B[0][n]);
        if(r<4){ RD128(B[1][r],xc[1],r*2048); }
        else if(r<12){ int mm=(r-4)>>1,t=(r-4)&1; RDTR(A[1][mm][t],wc[mm&1][t],8192+(mm>>1)*512); }
        ++r;
      }
    }
    if(PUB){ if(s==0){ WAITV(0); if(lane==0) __hip_atomic_store(flag,1u,RLX_AGENT); } }
    WAITL(0);
    BAR();
    {
      unsigned d = ((s&3)==3) ? (unsigned)(-3*SLOT) : (unsigned)SLOT;
      xc[0]+=d; xc[1]+=d; wc[0][0]+=d; wc[0][1]+=d; wc[1][0]+=d; wc[1][1]+=d;
    }
    {
      int r=0;
      #pragma unroll
      for(int m=0;m<4;++m)
      #pragma unroll
      for(int n=0;n<4;++n){
        M16(acc[m][n],cat(A[1][m][0],A[1][m][1]),B[1][n]);
        if(r<4){ RD128(B[0][r],xc[0],r*2048); }
        else if(r<12){ int mm=(r-4)>>1,t=(r-4)&1; RDTR(A[0][mm][t],wc[mm&1][t],(mm>>1)*512); }
        ++r;
      }
    }
    WAITL(0);
  }
}

__device__ __forceinline__ bool poll_all(GAS unsigned* p, bool active){
  for(unsigned spins=0;spins<SPIN_CAP;++spins){
    unsigned v = active ? __hip_atomic_load(p,RLX_AGENT) : 1u;
    if(__all(v!=0u)) return true;
    __builtin_amdgcn_s_sleep(1);
  }
  return false;
}

__global__ __launch_bounds__(512) void k_fused(Ptrs P){
  extern __shared__ __attribute__((aligned(16))) char smem[];
  const int lane=threadIdx.x&63, w=__builtin_amdgcn_readfirstlane(threadIdx.x>>6);
  const int b=blockIdx.x, g8=b&7, j=b>>3;
  const int br=g8>>2, oct=g8&3, jj=j&7, mb=j>>3;
  const int head=oct*8+jj;
  GAS unsigned* flags=(GAS unsigned*)(P.ws+FLAG_OFF);
  const int ogrp=(br*4+mb)*4+oct;
  const int rgrp=(br*4+mb)*8+jj;
  if(w>=4){
    const int lw=w-4;
    LAS char* L=(LAS char*)smem;
    const unsigned lb=(unsigned)(size_t)L;
    LST(0);
    const int rot=(4*mb+2*jj);
    const __amdgpu_buffer_rsrc_t rx=__builtin_amdgcn_make_buffer_rsrc((void*)((br?P.x1:P.x0)+(size_t)mb*128*1024),0,128*2048,0x00020000);
    const __amdgpu_buffer_rsrc_t rw=__builtin_amdgcn_make_buffer_rsrc((void*)((br?P.wq1:P.wq0)+head*128),0,1024*8192-head*256,0x00020000);
    const int vx=(lane>>3)*2048 + 16*((lane&7) ^ (((lane>>4)&3)|((lw&1)<<2)));
    const int X=((((lane>>4)&1)^(lw&1))<<1);
    const int vw=((lane>>2)&7)*8192 + (lane>>5)*64 + 16*((lane&3)^X);
    auto issue1=[&](int s){
      int st=(s+rot)&15; int slot=(s&3)*SLOT; int k0=st*64;
      #pragma unroll
      for(int u=0;u<4;++u){ int i=lw+4*u; __builtin_amdgcn_raw_ptr_buffer_load_lds(rx,(LAS void*)(L+slot+i*1024),16,vx,i*8*2048+k0*2,0,0); }
      #pragma unroll
      for(int u=0;u<4;++u){ int rg=lw+4*(u>>1), chh=u&1; int i=rg*2+chh; __builtin_amdgcn_raw_ptr_buffer_load_lds(rw,(LAS void*)(L+slot+16384+i*1024),16,vw,(k0+rg*8)*8192+chh*128,0,0); }
    };
    issue1(0); issue1(1);
    WAITV(8); BAR(); LST(1);
    issue1(2); issue1(3);
    const __amdgpu_buffer_rsrc_t rk=__builtin_amdgcn_make_buffer_rsrc((void*)((br?P.k1:P.k0)+(size_t)(mb*4*32+head)*4096),0,3*32*8192+8192,0x00020000);
    const __amdgpu_buffer_rsrc_t rv=__builtin_amdgcn_make_buffer_rsrc((void*)((br?P.v1:P.v0)+(size_t)(mb*4*32+head)*4096),0,3*32*8192+8192,0x00020000);
    const int vk=(lane>>4)*256 + 16*((lane&15) ^ (((lane>>4)<<2)|lw));
    v4u Kr[8], Vr[8];
    #pragma unroll 1
    for(int s=1;s<6;++s){ WAITV(16); BAR(); LST(1+s); issue1(s+3); }
    #pragma unroll
    for(int u=0;u<8;++u){ int so=(u>>1)*32*8192+(lw+4*(u&1))*1024; asm volatile("buffer_load_dwordx4 %0, %1, %2, %3 offen nt":"=v"(Kr[u]):"v"(vk),"s"(rk),"s"(so):"memory"); }
    #pragma unroll 1
    for(int s=6;s<9;++s){ WAITV(16); BAR(); LST(1+s); issue1(s+3); }
    #pragma unroll
    for(int u=0;u<8;++u){ int so=(u>>1)*32*8192+(lw+4*(u&1))*1024; asm volatile("buffer_load_dwordx4 %0, %1, %2, %3 offen nt":"=v"(Vr[u]):"v"(vk),"s"(rv),"s"(so):"memory"); }
    #pragma unroll 1
    for(int s=9;s<13;++s){ WAITV(16); BAR(); LST(1+s); issue1(s+3); }
    const __amdgpu_buffer_rsrc_t rwo=__builtin_amdgcn_make_buffer_rsrc((void*)((br?P.wo1:P.wo0)+(size_t)oct*1024*1024+jj*128),0,1024*2048-jj*256,0x00020000);
    const __amdgpu_buffer_rsrc_t ro=__builtin_amdgcn_make_buffer_rsrc((void*)(P.ws+O_OFF+((size_t)(br*4+mb)*64+oct*16)*16384),0,16*16384,0x00020000);
    const int vw2=((lane>>2)&7)*2048 + (lane>>5)*64 + 16*((lane&3)^X);
    auto st2=[&](int r)->int{
      if(r<2) return 2*jj+r;
      int t=((r-2)>>1)+2*mb; if(t>=7) t-=7; if(t>=7) t-=7;
      return 2*((jj+1+t)&7)+(r&1);
    };
    auto issue_wo=[&](int r){
      int slot=(r&3)*SLOT; int k0=st2(r)*64;
      #pragma unroll
      for(int u=0;u<4;++u){ int rg=lw+4*(u>>1), chh=u&1; int i=rg*2+chh; __builtin_amdgcn_raw_ptr_buffer_load_lds(rwo,(LAS void*)(L+slot+16384+i*1024),16,vw2,(k0+rg*8)*2048+chh*128,0,0); }
    };
    auto issue_o=[&](int r){
      int slot=(r&3)*SLOT; int so=st2(r)*16384;
      #pragma unroll
      for(int u=0;u<4;++u){ int i=lw+4*u; __builtin_amdgcn_raw_ptr_buffer_load_lds(ro,(LAS void*)(L+slot+i*1024),16,lane*16,so+i*1024,0,SC1); }
    };
    const unsigned kva=lb+lw*1024+lane*16;
    WAITV(16); BAR(); LST(14);
    #pragma unroll
    for(int u=0;u<8;++u){ asm volatile("":"+v"(Kr[u])); asm volatile("":"+v"(Vr[u])); }
    #pragma unroll
    for(int u=0;u<4;++u) WR128(kva,(u>>1)*8192+(u&1)*4096,Kr[u]);
    issue_wo(0);
    WAITV(12); BAR(); LST(15);
    #pragma unroll
    for(int u=4;u<8;++u) WR128(kva,SLOT+((u-4)>>1)*8192+(u&1)*4096,Kr[u]);
    issue_wo(1);
    WAITV(8); WAITL(0); BAR(); LST(16);
    #pragma unroll
    for(int u=0;u<4;++u) asm volatile("ds_write_b128 %0, %1 offset:%2"::"v"(kva+2*SLOT),"v"(Vr[u]),"i"((u>>1)*8192+(u&1)*4096):"memory");
    issue_wo(2);
    WAITL(0); BAR(); LST(17);
    #pragma unroll
    for(int u=4;u<8;++u) asm volatile("ds_write_b128 %0, %1 offset:%2"::"v"(kva+3*SLOT),"v"(Vr[u]),"i"(((u-4)>>1)*8192+(u&1)*4096):"memory");
    issue_wo(3);
    WAITL(0); BAR(); LST(18);
    WAITV(12); BAR(); LST(19);
    WAITV(8); BAR(); LST(20);
    issue_wo(4);
    poll_all(flags+OF_W+ogrp*32+(lane&31),(lane<32)&&((lane>>2)!=jj));
    LST(21);
    issue_o(2); issue_o(3); issue_o(4);
    WAITV(8); BAR(); LST(22);
    issue_o(5); issue_wo(5);
    WAITV(12); BAR(); LST(23);
    #pragma unroll 1
    for(int r=4;r<14;++r){ issue_o(r+2); issue_wo(r+2); WAITV(16); BAR(); }
    LST(24);
    WAITV(8); BAR();
    WAITV(0); BAR();
    LST(25);
    BAR();
    BAR();
    if(w==4&&lane<48) ((GAS unsigned*)(P.ws+(64u<<20)+blockIdx.x*256))[lane]=((volatile LAS unsigned*)((LAS char*)smem+LDS_BYTES))[lane];
    return;
  }
  const unsigned lb=(unsigned)(size_t)(LAS char*)smem;
  const int g=lane>>4, i16=lane&15, q=i16>>2, p=lane&3;
  const int wm=w>>1, wn=w&1;
  unsigned xc[2], wc[2][2];
  { int row=wm*64+i16; int s=(row>>1)&7; xc[0]=lb+128*row+16*((g)^s); xc[1]=lb+128*row+16*((4+g)^s); }
  #pragma unroll
  for(int c1=0;c1<2;++c1)
  #pragma unroll
  for(int t=0;t<2;++t){ int X=((t^g)&1)<<1; wc[c1][t]=lb+16384+2048*g+64*(4*t+q)+16*((2*c1+(p>>1))^X)+8*(p&1)+1024*wn; }
  f32x4 acc[4][4];
  #pragma unroll
  for(int m=0;m<4;++m)
  #pragma unroll
  for(int n=0;n<4;++n) acc[m][n]=(f32x4){0,0,0,0};
  BAR();
  CST(30);
  gemm_loop<false>(acc,xc,wc,nullptr,lane);
  CST(31);
  v2u Kf[2][2][2][2];
  {
    const int f=((i16&3)<<2)|((i16>>2)&3);
    #pragma unroll
    for(int mm=0;mm<2;++mm)
    #pragma unroll
    for(int t=0;t<2;++t){
      unsigned a=lb+wm*SLOT+256*i16+16*(((2*wn+mm)*4+2*t+(g>>1))^f)+8*(g&1);
      #pragma unroll
      for(int l=0;l<2;++l)
      #pragma unroll
      for(int kb=0;kb<2;++kb) RD64(Kf[l][kb][mm][t],a,l*8192+kb*4096);
    }
  }
  #pragma unroll
  for(int m=0;m<4;++m) FENCE4("s_nop 1",acc[m][0],acc[m][1],acc[m][2],acc[m][3]);
  v4u QB[4][2];
  #pragma unroll
  for(int n=0;n<4;++n)
  #pragma unroll
  for(int mm=0;mm<2;++mm){
    QB[n][mm]=(v4u){pk(acc[2*mm][n][0],acc[2*mm][n][1]),pk(acc[2*mm][n][2],acc[2*mm][n][3]),pk(acc[2*mm+1][n][0],acc[2*mm+1][n][1]),pk(acc[2*mm+1][n][2],acc[2*mm+1][n][3])};
  }
  WAITL(0);
  FENCE4("s_nop 1",QB[0][0],QB[0][1],QB[1][0],QB[1][1]);
  FENCE4("s_nop 1",QB[2][0],QB[2][1],QB[3][0],QB[3][1]);
  f32x4 S[2][2][2];
  #pragma unroll
  for(int l=0;l<2;++l)
  #pragma unroll
  for(int kb=0;kb<2;++kb)
  #pragma unroll
  for(int qb=0;qb<2;++qb) M16Z(S[l][kb][qb],cat(Kf[l][kb][0][0],Kf[l][kb][0][1]),QB[2*l+qb][0]);
  #pragma unroll
  for(int l=0;l<2;++l)
  #pragma unroll
  for(int kb=0;kb<2;++kb)
  #pragma unroll
  for(int qb=0;qb<2;++qb) M16(S[l][kb][qb],cat(Kf[l][kb][1][0],Kf[l][kb][1][1]),QB[2*l+qb][1]);
  FENCE4("s_nop 7",S[0][0][0],S[0][0][1],S[0][1][0],S[0][1][1]);
  FENCE4("s_nop 0",S[1][0][0],S[1][0][1],S[1][1][0],S[1][1][1]);
  {
    LAS char* sc=(LAS char*)smem+SCR+w*4096+lane*16;
    #pragma unroll
    for(int kb=0;kb<2;++kb)
    #pragma unroll
    for(int qb=0;qb<2;++qb) *(LAS f32x4*)(sc+(kb*2+qb)*1024) = wn? S[0][kb][qb] : S[1][kb][qb];
  }
  WAITL(0);
  asm volatile("":::"memory");
  BAR();
  CST(32);
  f32x4 T[2][2];
  {
    LAS char* sc=(LAS char*)smem+SCR+(w^1)*4096+lane*16;
    #pragma unroll
    for(int kb=0;kb<2;++kb)
    #pragma unroll
    for(int qb=0;qb<2;++qb){ f32x4 o=*(LAS f32x4*)(sc+(kb*2+qb)*1024); T[kb][qb]=(wn? S[1][kb][qb] : S[0][kb][qb]) + o; }
  }
  v2u Vf[8][2];
  {
    const int f=(q<<2)|g;
    #pragma unroll
    for(int u=0;u<4;++u){
      unsigned a=lb+(2+wm)*SLOT+wn*8192+256*(4*g+q)+16*((4*u+p)^f);
      #pragma unroll
      for(int hf=0;hf<2;++hf)
      #pragma unroll
      for(int t=0;t<2;++t) RDTR(Vf[2*u+hf][t],a,t*4096+hf*8);
    }
  }
  v4u PB[2];
  constexpr float SCL=0.08838834764831845f*1.4426950408889634f;
  #pragma unroll
  for(int qb=0;qb<2;++qb){
    float mx=fmaxf(fmaxf(fmaxf(T[0][qb][0],T[0][qb][1]),fmaxf(T[0][qb][2],T[0][qb][3])),fmaxf(fmaxf(T[1][qb][0],T[1][qb][1]),fmaxf(T[1][qb][2],T[1][qb][3])));
    { auto r=__builtin_amdgcn_permlane16_swap(__float_as_uint(mx),__float_as_uint(mx),false,false); mx=fmaxf(__uint_as_float(r[0]),__uint_as_float(r[1])); }
    { auto r=__builtin_amdgcn_permlane32_swap(__float_as_uint(mx),__float_as_uint(mx),false,false); mx=fmaxf(__uint_as_float(r[0]),__uint_as_float(r[1])); }
    float e[8]; float sum=0; const float nm=-mx*SCL;
    #pragma unroll
    for(int kb=0;kb<2;++kb)
    #pragma unroll
    for(int c=0;c<4;++c){ e[kb*4+c]=__builtin_amdgcn_exp2f(__builtin_fmaf(T[kb][qb][c],SCL,nm)); sum+=e[kb*4+c]; }
    { auto r=__builtin_amdgcn_permlane16_swap(__float_as_uint(sum),__float_as_uint(sum),false,false); sum=__uint_as_float(r[0])+__uint_as_float(r[1]); }
    { auto r=__builtin_amdgcn_permlane32_swap(__float_as_uint(sum),__float_as_uint(sum),false,false); sum=__uint_as_float(r[0])+__uint_as_float(r[1]); }
    float inv=__builtin_amdgcn_rcpf(sum);
    PB[qb]=(v4u){pk(e[0]*inv,e[1]*inv),pk(e[2]*inv,e[3]*inv),pk(e[4]*inv,e[5]*inv),pk(e[6]*inv,e[7]*inv)};
  }
  WAITL(0);
  asm volatile("s_nop 1":"+v"(PB[0]),"+v"(PB[1]));
  f32x4 O[8][2];
  #pragma unroll
  for(int m=0;m<8;++m)
  #pragma unroll
  for(int qb=0;qb<2;++qb) M16Z(O[m][qb],cat(Vf[m][0],Vf[m][1]),PB[qb]);
  FENCE4("s_nop 7",O[6][0],O[6][1],O[7][0],O[7][1]);
  #pragma unroll
  for(int m=0;m<6;m+=2) FENCE4("",O[m][0],O[m][1],O[m+1][0],O[m+1][1]);
  {
    const __amdgpu_buffer_rsrc_t rs=__builtin_amdgcn_make_buffer_rsrc((void*)(P.ws+O_OFF+((size_t)(br*4+mb)*64+2*head)*16384),0,2*16384,0x00020000);
    const int bb=2*wm+wn;
    const unsigned oa=lb+bb*4096+lane*16;
    #pragma unroll
    for(int mm=0;mm<4;++mm)
    #pragma unroll
    for(int qb=0;qb<2;++qb){
      v4u o=(v4u){pk(O[2*mm][qb][0],O[2*mm][qb][1]),pk(O[2*mm][qb][2],O[2*mm][qb][3]),pk(O[2*mm+1][qb][0],O[2*mm+1][qb][1]),pk(O[2*mm+1][qb][2],O[2*mm+1][qb][3])};
      __builtin_amdgcn_raw_buffer_store_b128(o,rs,bb*4096+lane*16,(mm>>1)*16384+qb*2048+(mm&1)*1024,SC1);
      WR128(oa,(mm>>1)*SLOT+qb*2048+(mm&1)*1024,o);
    }
  }
  xc[0]=lb+lane*16+wm*8192; xc[1]=xc[0]+1024;
  #pragma unroll
  for(int c1=0;c1<2;++c1)
  #pragma unroll
  for(int t=0;t<2;++t){ int X=((t^g)&1)<<1; wc[c1][t]=lb+16384+2048*g+64*(4*t+q)+16*((2*c1+(p>>1))^X)+8*(p&1)+1024*wn; }
  #pragma unroll
  for(int m=0;m<4;++m)
  #pragma unroll
  for(int n=0;n<4;++n) acc[m][n]=(f32x4){0,0,0,0};
  WAITL(0);
  CST(33);
  BAR();
  CST(34);
  gemm_loop<true>(acc,xc,wc,flags+OF_W+ogrp*32+jj*4+w,lane);
  CST(35);
  #pragma unroll
  for(int m=0;m<4;++m) FENCE4("s_nop 1",acc[m][0],acc[m][1],acc[m][2],acc[m][3]);
  {
    constexpr int TB = PART_BF16? 512:1024;
    const __amdgpu_buffer_rsrc_t rp=__builtin_amdgcn_make_buffer_rsrc((void*)(P.ws+PART_OFF+(size_t)rgrp*(4*4*4*4*TB)),0,4*4*4*4*TB,0x00020000);
    #pragma unroll
    for(int n=0;n<4;++n){
      if(n!=oct){
        #pragma unroll
        for(int m=0;m<4;m+=(PART_BF16?2:1)){
          if(PART_BF16){
            v4u o=(v4u){pk(acc[m][n][0],acc[m][n][1]),pk(acc[m][n][2],acc[m][n][3]),pk(acc[m+1][n][0],acc[m+1][n][1]),pk(acc[m+1][n][2],acc[m+1][n][3])};
            __builtin_amdgcn_raw_buffer_store_b128(o,rp,lane*16,(((n*4+oct)*4+w)*4+m)*TB,SC1);
          } else {
            __builtin_amdgcn_raw_buffer_store_b128(__builtin_bit_cast(v4u,acc[m][n]),rp,lane*16,(((n*4+oct)*4+w)*4+m)*TB,SC1);
          }
        }
      }
    }
    WAITV(0);
    if(lane==0) __hip_atomic_store(flags+RF_W+rgrp*32+oct*4+w,1u,RLX_AGENT);
    CST(36);
    f32x4 mine[4];
    #pragma unroll
    for(int m=0;m<4;++m) mine[m]= oct==0?acc[m][0]: oct==1?acc[m][1]: oct==2?acc[m][2]:acc[m][3];
    poll_all(flags+RF_W+rgrp*32+(lane&3)*4+w,(lane<4)&&(lane!=oct));
    CST(37);
    v4u in[3][PART_BF16?2:4];
    #pragma unroll
    for(int e=0;e<3;++e){
      int src=e+(e>=oct?1:0);
      #pragma unroll
      for(int m=0;m<(PART_BF16?2:4);++m) in[e][m]=__builtin_amdgcn_raw_buffer_load_b128(rp,lane*16,(((oct*4+src)*4+w)*4+(PART_BF16?2*m:m))*TB,SC1);
    }
    #pragma unroll
    for(int e=0;e<3;++e){
      if(PART_BF16){
        #pragma unroll
        for(int m=0;m<2;++m){
          mine[2*m][0]+=bflo(in[e][m][0]); mine[2*m][1]+=bfhi(in[e][m][0]); mine[2*m][2]+=bflo(in[e][m][1]); mine[2*m][3]+=bfhi(in[e][m][1]);
          mine[2*m+1][0]+=bflo(in[e][m][2]); mine[2*m+1][1]+=bfhi(in[e][m][2]); mine[2*m+1][2]+=bflo(in[e][m][3]); mine[2*m+1][3]+=bfhi(in[e][m][3]);
        }
      } else {
        #pragma unroll
        for(int m=0;m<4;++m) mine[m]+=__builtin_bit_cast(f32x4,in[e][m]);
      }
    }
    u16* op=P.out+(size_t)(mb*128+wm*64+16*oct+i16)*2048+br*1024+jj*128+wn*64+4*g;
    #pragma unroll
    for(int m=0;m<4;++m) *(GAS v2u*)(op+16*m)=(v2u){pk(mine[m][0],mine[m][1]),pk(mine[m][2],mine[m][3])};
    CST(38);
  }
  BAR();
}

__global__ void k_spin(Ptrs P,int idx,int mult){
  const GAS unsigned* h=(const GAS unsigned*)(P.ws+(64u<<20));
  unsigned t0v=h[threadIdx.x*64+0], tv=h[threadIdx.x*64+idx];
  __shared__ unsigned a[256],c[256];
  a[threadIdx.x]=t0v; c[threadIdx.x]=tv; __syncthreads();
  if(threadIdx.x==0){
    unsigned mn=a[0]; for(int i=1;i<256;++i) if((int)(a[i]-mn)<0) mn=a[i];
    unsigned mx=0; for(int i=0;i<256;++i){ unsigned d=c[i]-mn; if(d>mx) mx=d; }
    unsigned lim=mx*mult; if(lim>100000u) lim=100000u;
    unsigned long long r0,r; asm volatile("s_memrealtime %0\n\ts_waitcnt lgkmcnt(0)":"=s"(r0)::"memory");
    do{ asm volatile("s_memrealtime %0\n\ts_waitcnt lgkmcnt(0)":"=s"(r)::"memory"); }while((unsigned)(r-r0)<lim);
  }
}
extern "C" void kernel_launch(void* const* d_in, const int* in_sizes, int n_in,
                              void* d_out, int out_size, void* d_ws, size_t ws_size,
                              hipStream_t stream) {
  Ptrs P;
  P.x0=(const u16*)d_in[0]; P.k0=(const u16*)d_in[1]; P.v0=(const u16*)d_in[2]; P.wq0=(const u16*)d_in[3]; P.wo0=(const u16*)d_in[4];
  P.x1=(const u16*)d_in[5]; P.k1=(const u16*)d_in[6]; P.v1=(const u16*)d_in[7]; P.wq1=(const u16*)d_in[8]; P.wo1=(const u16*)d_in[9];
  P.out=(u16*)d_out; P.ws=(char*)d_ws;
  const int lds=LDS_BYTES+256;
  (void)hipFuncSetAttribute((const void*)k_fused,hipFuncAttributeMaxDynamicSharedMemorySize,lds);
  (void)hipMemsetAsync((char*)d_ws+FLAG_OFF,0,FLAG_BYTES,stream);
  k_fused<<<256,512,lds,stream>>>(P);
  k_spin<<<1,256,0,stream>>>(P,0,0);
}
```

```cpp
#include <hip/hip_runtime.h>
#include <hip/hip_bf16.h>
typedef unsigned v4u __attribute__((ext_vector_type(4)));
typedef unsigned v2u __attribute__((ext_vector_type(2)));
typedef float f32x4 __attribute__((ext_vector_type(4)));
typedef unsigned short u16;
#define GAS __attribute__((address_space(1)))
#define LAS __attribute__((address_space(3)))
#define M16(c,a,b) asm volatile("v_mfma_f32_16x16x32_bf16 %0, %1, %2, %0":"+v"(c):"v"(a),"v"(b))
#define M16Z(c,a,b) asm volatile("v_mfma_f32_16x16x32_bf16 %0, %1, %2, 0":"=v"(c):"v"(a),"v"(b))
#define FENCE4(n,a,b,c,d) asm volatile(n:"+v"(a),"+v"(b),"+v"(c),"+v"(d))
#define RD128(d,addr,off) asm volatile("ds_read_b128 %0, %1 offset:%2":"=v"(d):"v"(addr),"i"(off))
#define RD64(d,addr,off) asm volatile("ds_read_b64 %0, %1 offset:%2":"=v"(d):"v"(addr),"i"(off))
#define RDTR(d,addr,off) asm volatile("ds_read_b64_tr_b16 %0, %1 offset:%2":"=v"(d):"v"(addr),"i"(off))
#define WR128(addr,off,d) asm volatile("ds_write_b128 %0, %1 offset:%2"::"v"(addr),"v"(d),"i"(off):"memory")
#define WAITL(n) asm volatile("s_waitcnt lgkmcnt(%0)"::"i"(n):"memory")
#define WAITV(n) asm volatile("s_waitcnt vmcnt(%0)"::"i"(n):"memory")
#define BAR() asm volatile("s_barrier":::"memory")
#define RLX_AGENT __ATOMIC_RELAXED, __HIP_MEMORY_SCOPE_AGENT
#define AUX_KV 2
#define PART_BF16 1
constexpr int SC1=16;
constexpr int SLOT=32768;
constexpr int SCR=4*SLOT;
constexpr int LDS_BYTES=SCR+16384;
constexpr size_t O_OFF=0;
constexpr size_t PART_OFF=(size_t)8<<20;
constexpr size_t FLAG_OFF=(size_t)32<<20;
constexpr int OF_W=0;
constexpr int RF_W=32*32;
constexpr int FLAG_BYTES=(32*32+64*32)*4;
constexpr unsigned SPIN_CAP=1u<<16;

#define CST(i)
#define LST(i)

__device__ __forceinline__ v4u cat(v2u a,v2u b){ return __builtin_shufflevector(a,b,0,1,2,3); }
__device__ __forceinline__ unsigned pk(float a,float b){ unsigned r; asm("v_cvt_pk_bf16_f32 %0, %1, %2":"=v"(r):"v"(a),"v"(b)); return r; }
__device__ __forceinline__ float bflo(unsigned u){ return __uint_as_float(u<<16); }
__device__ __forceinline__ float bfhi(unsigned u){ return __uint_as_float(u&0xffff0000u); }

struct Ptrs { const u16 *x0,*x1,*k0,*k1,*v0,*v1,*wq0,*wq1,*wo0,*wo1; u16* out; char* ws; };

template<bool PUB>
__device__ __forceinline__ void gemm_loop(f32x4 (&acc)[4][4], unsigned (&xc)[2], unsigned (&wc)[2][2], GAS unsigned* flag, int lane){
  v2u A[2][4][2]; v4u B[2][4];
  #pragma unroll
  for(int n=0;n<4;++n) RD128(B[0][n],xc[0],n*2048);
  #pragma unroll
  for(int m=0;m<4;++m){ RDTR(A[0][m][0],wc[m&1][0],(m>>1)*512); RDTR(A[0][m][1],wc[m&1][1],(m>>1)*512); }
  WAITL(0);
  #pragma unroll 1
  for(int s=0;s<16;++s){
    {
      int r=0;
      #pragma unroll
      for(int m=0;m<4;++m)
      #pragma unroll
      for(int n=0;n<4;++n){
        M16(acc[m][n],cat(A[0][m][0],A[0][m][1]),B[0][n]);
        if(r<4){ RD128(B[1][r],xc[1],r*2048); }
        else if(r<12){ int mm=(r-4)>>1,t=(r-4)&1; RDTR(A[1][mm][t],wc[mm&1][t],8192+(mm>>1)*512); }
        ++r;
      }
    }
    if(PUB){ if(s==0){ WAITV(0); if(lane==0) __hip_atomic_store(flag,1u,RLX_AGENT); } }
    WAITL(0);
    BAR();
    {
      unsigned d = ((s&3)==3) ? (unsigned)(-3*SLOT) : (unsigned)SLOT;
      xc[0]+=d; xc[1]+=d; wc[0][0]+=d; wc[0][1]+=d; wc[1][0]+=d; wc[1][1]+=d;
    }
    {
      int r=0;
      #pragma unroll
      for(int m=0;m<4;++m)
      #pragma unroll
      for(int n=0;n<4;++n){
        M16(acc[m][n],cat(A[1][m][0],A[1][m][1]),B[1][n]);
        if(r<4){ RD128(B[0][r],xc[0],r*2048); }
        else if(r<12){ int mm=(r-4)>>1,t=(r-4)&1; RDTR(A[0][mm][t],wc[mm&1][t],(mm>>1)*512); }
        ++r;
      }
    }
    WAITL(0);
  }
}

__device__ __forceinline__ bool poll_all(GAS unsigned* p, bool active){
  for(unsigned spins=0;spins<SPIN_CAP;++spins){
    unsigned v = active ? __hip_atomic_load(p,RLX_AGENT) : 1u;
    if(__all(v!=0u)) return true;
    __builtin_amdgcn_s_sleep(1);
  }
  return false;
}

__global__ __launch_bounds__(512) void k_fused(Ptrs P){
  extern __shared__ __attribute__((aligned(16))) char smem[];
  const int lane=threadIdx.x&63, w=__builtin_amdgcn_readfirstlane(threadIdx.x>>6);
  const int b=blockIdx.x, g8=b&7, j=b>>3;
  const int br=g8>>2, oct=g8&3, jj=j&7, mb=j>>3;
  const int head=oct*8+jj;
  GAS unsigned* flags=(GAS unsigned*)(P.ws+FLAG_OFF);
  const int ogrp=(br*4+mb)*4+oct;
  const int rgrp=(br*4+mb)*8+jj;
  if(w>=4){
    const int lw=w-4;
    LAS char* L=(LAS char*)smem;
    const unsigned lb=(unsigned)(size_t)L;
    LST(0);
    const int rot=(4*mb+2*jj);
    const __amdgpu_buffer_rsrc_t rx=__builtin_amdgcn_make_buffer_rsrc((void*)((br?P.x1:P.x0)+(size_t)mb*128*1024),0,128*2048,0x00020000);
    const __amdgpu_buffer_rsrc_t rw=__builtin_amdgcn_make_buffer_rsrc((void*)((br?P.wq1:P.wq0)+head*128),0,1024*8192-head*256,0x00020000);
    const int vx=(lane>>3)*2048 + 16*((lane&7) ^ (((lane>>4)&3)|((lw&1)<<2)));
    const int X=((((lane>>4)&1)^(lw&1))<<1);
    const int vw=((lane>>2)&7)*8192 + (lane>>5)*64 + 16*((lane&3)^X);
    auto issue1=[&](int s){
      int st=(s+rot)&15; int slot=(s&3)*SLOT; int k0=st*64;
      #pragma unroll
      for(int u=0;u<4;++u){ int i=lw+4*u; __builtin_amdgcn_raw_ptr_buffer_load_lds(rx,(LAS void*)(L+slot+i*1024),16,vx,i*8*2048+k0*2,0,0); }
      #pragma unroll
      for(int u=0;u<4;++u){ int rg=lw+4*(u>>1), chh=u&1; int i=rg*2+chh; __builtin_amdgcn_raw_ptr_buffer_load_lds(rw,(LAS void*)(L+slot+16384+i*1024),16,vw,(k0+rg*8)*8192+chh*128,0,0); }
    };
    issue1(0); issue1(1);
    WAITV(8); BAR(); LST(1);
    issue1(2); issue1(3);
    const __amdgpu_buffer_rsrc_t rk=__builtin_amdgcn_make_buffer_rsrc((void*)((br?P.k1:P.k0)+(size_t)(mb*4*32+head)*4096),0,3*32*8192+8192,0x00020000);
    const __amdgpu_buffer_rsrc_t rv=__builtin_amdgcn_make_buffer_rsrc((void*)((br?P.v1:P.v0)+(size_t)(mb*4*32+head)*4096),0,3*32*8192+8192,0x00020000);
    const int vk=(lane>>4)*256 + 16*((lane&15) ^ (((lane>>4)<<2)|lw));
    v4u Kr[8], Vr[8];
    #pragma unroll 1
    for(int s=1;s<6;++s){ WAITV(16); BAR(); LST(1+s); issue1(s+3); }
    #pragma unroll
    for(int u=0;u<8;++u){ int so=(u>>1)*32*8192+(lw+4*(u&1))*1024; asm volatile("buffer_load_dwordx4 %0, %1, %2, %3 offen nt":"=v"(Kr[u]):"v"(vk),"s"(rk),"s"(so):"memory"); }
    #pragma unroll 1
    for(int s=6;s<9;++s){ WAITV(16); BAR(); LST(1+s); issue1(s+3); }
    #pragma unroll
    for(int u=0;u<8;++u){ int so=(u>>1)*32*8192+(lw+4*(u&1))*1024; asm volatile("buffer_load_dwordx4 %0, %1, %2, %3 offen nt":"=v"(Vr[u]):"v"(vk),"s"(rv),"s"(so):"memory"); }
    #pragma unroll 1
    for(int s=9;s<13;++s){ WAITV(16); BAR(); LST(1+s); issue1(s+3); }
    const __amdgpu_buffer_rsrc_t rwo=__builtin_amdgcn_make_buffer_rsrc((void*)((br?P.wo1:P.wo0)+(size_t)oct*1024*1024+jj*128),0,1024*2048-jj*256,0x00020000);
    const __amdgpu_buffer_rsrc_t ro=__builtin_amdgcn_make_buffer_rsrc((void*)(P.ws+O_OFF+((size_t)(br*4+mb)*64+oct*16)*16384),0,16*16384,0x00020000);
    const int vw2=((lane>>2)&7)*2048 + (lane>>5)*64 + 16*((lane&3)^X);
    auto st2=[&](int r)->int{
      if(r<2) return 2*jj+r;
      int t=((r-2)>>1)+2*mb; if(t>=7) t-=7; if(t>=7) t-=7;
      return 2*((jj+1+t)&7)+(r&1);
    };
    auto issue_wo=[&](int r){
      int slot=(r&3)*SLOT; int k0=st2(r)*64;
      #pragma unroll
      for(int u=0;u<4;++u){ int rg=lw+4*(u>>1), chh=u&1; int i=rg*2+chh; __builtin_amdgcn_raw_ptr_buffer_load_lds(rwo,(LAS void*)(L+slot+16384+i*1024),16,vw2,(k0+rg*8)*2048+chh*128,0,0); }
    };
    auto issue_o=[&](int r){
      int slot=(r&3)*SLOT; int so=st2(r)*16384;
      #pragma unroll
      for(int u=0;u<4;++u){ int i=lw+4*u; __builtin_amdgcn_raw_ptr_buffer_load_lds(ro,(LAS void*)(L+slot+i*1024),16,lane*16,so+i*1024,0,SC1); }
    };
    const unsigned kva=lb+lw*1024+lane*16;
    WAITV(16); BAR(); LST(14);
    #pragma unroll
    for(int u=0;u<8;++u){ asm volatile("":"+v"(Kr[u])); asm volatile("":"+v"(Vr[u])); }
    #pragma unroll
    for(int u=0;u<4;++u) WR128(kva,(u>>1)*8192+(u&1)*4096,Kr[u]);
    issue_wo(0);
    WAITV(12); BAR(); LST(15);
    #pragma unroll
    for(int u=4;u<8;++u) WR128(kva,SLOT+((u-4)>>1)*8192+(u&1)*4096,Kr[u]);
    issue_wo(1);
    WAITV(8); WAITL(0); BAR(); LST(16);
    #pragma unroll
    for(int u=0;u<4;++u) asm volatile("ds_write_b128 %0, %1 offset:%2"::"v"(kva+2*SLOT),"v"(Vr[u]),"i"((u>>1)*8192+(u&1)*4096):"memory");
    issue_wo(2);
    WAITL(0); BAR(); LST(17);
    #pragma unroll
    for(int u=4;u<8;++u) asm volatile("ds_write_b128 %0, %1 offset:%2"::"v"(kva+3*SLOT),"v"(Vr[u]),"i"(((u-4)>>1)*8192+(u&1)*4096):"memory");
    issue_wo(3);
    WAITL(0); BAR(); LST(18);
    WAITV(12); BAR(); LST(19);
    WAITV(8); BAR(); LST(20);
    issue_wo(4);
    poll_all(flags+OF_W+ogrp*32+(lane&31),(lane<32)&&((lane>>2)!=jj));
    LST(21);
    issue_o(2); issue_o(3); issue_o(4);
    WAITV(8); BAR(); LST(22);
    issue_o(5); issue_wo(5);
    WAITV(12); BAR(); LST(23);
    #pragma unroll 1
    for(int r=4;r<14;++r){ issue_o(r+2); issue_wo(r+2); WAITV(16); BAR(); }
    LST(24);
    WAITV(8); BAR();
    WAITV(0); BAR();
    LST(25);
    return;
  }
  const unsigned lb=(unsigned)(size_t)(LAS char*)smem;
  const int g=lane>>4, i16=lane&15, q=i16>>2, p=lane&3;
  const int wm=w>>1, wn=w&1;
  unsigned xc[2], wc[2][2];
  { int row=wm*64+i16; int s=(row>>1)&7; xc[0]=lb+128*row+16*((g)^s); xc[1]=lb+128*row+16*((4+g)^s); }
  #pragma unroll
  for(int c1=0;c1<2;++c1)
  #pragma unroll
  for(int t=0;t<2;++t){ int X=((t^g)&1)<<1; wc[c1][t]=lb+16384+2048*g+64*(4*t+q)+16*((2*c1+(p>>1))^X)+8*(p&1)+1024*wn; }
  f32x4 acc[4][4];
  #pragma unroll
  for(int m=0;m<4;++m)
  #pragma unroll
  for(int n=0;n<4;++n) acc[m][n]=(f32x4){0,0,0,0};
  BAR();
  CST(30);
  gemm_loop<false>(acc,xc,wc,nullptr,lane);
  CST(31);
  v2u Kf[2][2][2][2];
  {
    const int f=((i16&3)<<2)|((i16>>2)&3);
    #pragma unroll
    for(int mm=0;mm<2;++mm)
    #pragma unroll
    for(int t=0;t<2;++t){
      unsigned a=lb+wm*SLOT+256*i16+16*(((2*wn+mm)*4+2*t+(g>>1))^f)+8*(g&1);
      #pragma unroll
      for(int l=0;l<2;++l)
      #pragma unroll
      for(int kb=0;kb<2;++kb) RD64(Kf[l][kb][mm][t],a,l*8192+kb*4096);
    }
  }
  #pragma unroll
  for(int m=0;m<4;++m) FENCE4("s_nop 1",acc[m][0],acc[m][1],acc[m][2],acc[m][3]);
  v4u QB[4][2];
  #pragma unroll
  for(int n=0;n<4;++n)
  #pragma unroll
  for(int mm=0;mm<2;++mm){
    QB[n][mm]=(v4u){pk(acc[2*mm][n][0],acc[2*mm][n][1]),pk(acc[2*mm][n][2],acc[2*mm][n][3]),pk(acc[2*mm+1][n][0],acc[2*mm+1][n][1]),pk(acc[2*mm+1][n][2],acc[2*mm+1][n][3])};
  }
  WAITL(0);
  FENCE4("s_nop 1",QB[0][0],QB[0][1],QB[1][0],QB[1][1]);
  FENCE4("s_nop 1",QB[2][0],QB[2][1],QB[3][0],QB[3][1]);
  f32x4 S[2][2][2];
  #pragma unroll
  for(int l=0;l<2;++l)
  #pragma unroll
  for(int kb=0;kb<2;++kb)
  #pragma unroll
  for(int qb=0;qb<2;++qb) M16Z(S[l][kb][qb],cat(Kf[l][kb][0][0],Kf[l][kb][0][1]),QB[2*l+qb][0]);
  #pragma unroll
  for(int l=0;l<2;++l)
  #pragma unroll
  for(int kb=0;kb<2;++kb)
  #pragma unroll
  for(int qb=0;qb<2;++qb) M16(S[l][kb][qb],cat(Kf[l][kb][1][0],Kf[l][kb][1][1]),QB[2*l+qb][1]);
  FENCE4("s_nop 7",S[0][0][0],S[0][0][1],S[0][1][0],S[0][1][1]);
  FENCE4("s_nop 0",S[1][0][0],S[1][0][1],S[1][1][0],S[1][1][1]);
  {
    LAS char* sc=(LAS char*)smem+SCR+w*4096+lane*16;
    #pragma unroll
    for(int kb=0;kb<2;++kb)
    #pragma unroll
    for(int qb=0;qb<2;++qb) *(LAS f32x4*)(sc+(kb*2+qb)*1024) = wn? S[0][kb][qb] : S[1][kb][qb];
  }
  WAITL(0);
  asm volatile("":::"memory");
  BAR();
  CST(32);
  f32x4 T[2][2];
  {
    LAS char* sc=(LAS char*)smem+SCR+(w^1)*4096+lane*16;
    #pragma unroll
    for(int kb=0;kb<2;++kb)
    #pragma unroll
    for(int qb=0;qb<2;++qb){ f32x4 o=*(LAS f32x4*)(sc+(kb*2+qb)*1024); T[kb][qb]=(wn? S[1][kb][qb] : S[0][kb][qb]) + o; }
  }
  v2u Vf[8][2];
  {
    const int f=(q<<2)|g;
    #pragma unroll
    for(int u=0;u<4;++u){
      unsigned a=lb+(2+wm)*SLOT+wn*8192+256*(4*g+q)+16*((4*u+p)^f);
      #pragma unroll
      for(int hf=0;hf<2;++hf)
      #pragma unroll
      for(int t=0;t<2;++t) RDTR(Vf[2*u+hf][t],a,t*4096+hf*8);
    }
  }
  v4u PB[2];
  constexpr float SCL=0.08838834764831845f*1.4426950408889634f;
  #pragma unroll
  for(int qb=0;qb<2;++qb){
    float mx=fmaxf(fmaxf(fmaxf(T[0][qb][0],T[0][qb][1]),fmaxf(T[0][qb][2],T[0][qb][3])),fmaxf(fmaxf(T[1][qb][0],T[1][qb][1]),fmaxf(T[1][qb][2],T[1][qb][3])));
    { auto r=__builtin_amdgcn_permlane16_swap(__float_as_uint(mx),__float_as_uint(mx),false,false); mx=fmaxf(__uint_as_float(r[0]),__uint_as_float(r[1])); }
    { auto r=__builtin_amdgcn_permlane32_swap(__float_as_uint(mx),__float_as_uint(mx),false,false); mx=fmaxf(__uint_as_float(r[0]),__uint_as_float(r[1])); }
    float e[8]; float sum=0; const float nm=-mx*SCL;
    #pragma unroll
    for(int kb=0;kb<2;++kb)
    #pragma unroll
    for(int c=0;c<4;++c){ e[kb*4+c]=__builtin_amdgcn_exp2f(__builtin_fmaf(T[kb][qb][c],SCL,nm)); sum+=e[kb*4+c]; }
    { auto r=__builtin_amdgcn_permlane16_swap(__float_as_uint(sum),__float_as_uint(sum),false,false); sum=__uint_as_float(r[0])+__uint_as_float(r[1]); }
    { auto r=__builtin_amdgcn_permlane32_swap(__float_as_uint(sum),__float_as_uint(sum),false,false); sum=__uint_as_float(r[0])+__uint_as_float(r[1]); }
    float inv=__builtin_amdgcn_rcpf(sum);
    PB[qb]=(v4u){pk(e[0]*inv,e[1]*inv),pk(e[2]*inv,e[3]*inv),pk(e[4]*inv,e[5]*inv),pk(e[6]*inv,e[7]*inv)};
  }
  WAITL(0);
  asm volatile("s_nop 1":"+v"(PB[0]),"+v"(PB[1]));
  f32x4 O[8][2];
  #pragma unroll
  for(int m=0;m<8;++m)
  #pragma unroll
  for(int qb=0;qb<2;++qb) M16Z(O[m][qb],cat(Vf[m][0],Vf[m][1]),PB[qb]);
  FENCE4("s_nop 7",O[6][0],O[6][1],O[7][0],O[7][1]);
  #pragma unroll
  for(int m=0;m<6;m+=2) FENCE4("",O[m][0],O[m][1],O[m+1][0],O[m+1][1]);
  {
    const __amdgpu_buffer_rsrc_t rs=__builtin_amdgcn_make_buffer_rsrc((void*)(P.ws+O_OFF+((size_t)(br*4+mb)*64+2*head)*16384),0,2*16384,0x00020000);
    const int bb=2*wm+wn;
    const unsigned oa=lb+bb*4096+lane*16;
    #pragma unroll
    for(int mm=0;mm<4;++mm)
    #pragma unroll
    for(int qb=0;qb<2;++qb){
      v4u o=(v4u){pk(O[2*mm][qb][0],O[2*mm][qb][1]),pk(O[2*mm][qb][2],O[2*mm][qb][3]),pk(O[2*mm+1][qb][0],O[2*mm+1][qb][1]),pk(O[2*mm+1][qb][2],O[2*mm+1][qb][3])};
      __builtin_amdgcn_raw_buffer_store_b128(o,rs,bb*4096+lane*16,(mm>>1)*16384+qb*2048+(mm&1)*1024,SC1);
      WR128(oa,(mm>>1)*SLOT+qb*2048+(mm&1)*1024,o);
    }
  }
  xc[0]=lb+lane*16+wm*8192; xc[1]=xc[0]+1024;
  #pragma unroll
  for(int c1=0;c1<2;++c1)
  #pragma unroll
  for(int t=0;t<2;++t){ int X=((t^g)&1)<<1; wc[c1][t]=lb+16384+2048*g+64*(4*t+q)+16*((2*c1+(p>>1))^X)+8*(p&1)+1024*wn; }
  #pragma unroll
  for(int m=0;m<4;++m)
  #pragma unroll
  for(int n=0;n<4;++n) acc[m][n]=(f32x4){0,0,0,0};
  WAITL(0);
  CST(33);
  BAR();
  CST(34);
  gemm_loop<true>(acc,xc,wc,flags+OF_W+ogrp*32+jj*4+w,lane);
  CST(35);
  #pragma unroll
  for(int m=0;m<4;++m) FENCE4("s_nop 1",acc[m][0],acc[m][1],acc[m][2],acc[m][3]);
  {
    constexpr int TB = PART_BF16? 512:1024;
    const __amdgpu_buffer_rsrc_t rp=__builtin_amdgcn_make_buffer_rsrc((void*)(P.ws+PART_OFF+(size_t)rgrp*(4*4*4*4*TB)),0,4*4*4*4*TB,0x00020000);
    #pragma unroll
    for(int n=0;n<4;++n){
      if(n!=oct){
        #pragma unroll
        for(int m=0;m<4;m+=(PART_BF16?2:1)){
          if(PART_BF16){
            v4u o=(v4u){pk(acc[m][n][0],acc[m][n][1]),pk(acc[m][n][2],acc[m][n][3]),pk(acc[m+1][n][0],acc[m+1][n][1]),pk(acc[m+1][n][2],acc[m+1][n][3])};
            __builtin_amdgcn_raw_buffer_store_b128(o,rp,lane*16,(((n*4+oct)*4+w)*4+m)*TB,SC1);
          } else {
            __builtin_amdgcn_raw_buffer_store_b128(__builtin_bit_cast(v4u,acc[m][n]),rp,lane*16,(((n*4+oct)*4+w)*4+m)*TB,SC1);
          }
        }
      }
    }
    WAITV(0);
    if(lane==0) __hip_atomic_store(flags+RF_W+rgrp*32+oct*4+w,1u,RLX_AGENT);
    CST(36);
    f32x4 mine[4];
    #pragma unroll
    for(int m=0;m<4;++m) mine[m]= oct==0?acc[m][0]: oct==1?acc[m][1]: oct==2?acc[m][2]:acc[m][3];
    poll_all(flags+RF_W+rgrp*32+(lane&3)*4+w,(lane<4)&&(lane!=oct));
    CST(37);
    v4u in[3][PART_BF16?2:4];
    #pragma unroll
    for(int e=0;e<3;++e){
      int src=e+(e>=oct?1:0);
      #pragma unroll
      for(int m=0;m<(PART_BF16?2:4);++m) in[e][m]=__builtin_amdgcn_raw_buffer_load_b128(rp,lane*16,(((oct*4+src)*4+w)*4+(PART_BF16?2*m:m))*TB,SC1);
    }
    #pragma unroll
    for(int e=0;e<3;++e){
      if(PART_BF16){
        #pragma unroll
        for(int m=0;m<2;++m){
          mine[2*m][0]+=bflo(in[e][m][0]); mine[2*m][1]+=bfhi(in[e][m][0]); mine[2*m][2]+=bflo(in[e][m][1]); mine[2*m][3]+=bfhi(in[e][m][1]);
          mine[2*m+1][0]+=bflo(in[e][m][2]); mine[2*m+1][1]+=bfhi(in[e][m][2]); mine[2*m+1][2]+=bflo(in[e][m][3]); mine[2*m+1][3]+=bfhi(in[e][m][3]);
        }
      } else {
        #pragma unroll
        for(int m=0;m<4;++m) mine[m]+=__builtin_bit_cast(f32x4,in[e][m]);
      }
    }
    u16* op=P.out+(size_t)(mb*128+wm*64+16*oct+i16)*2048+br*1024+jj*128+wn*64+4*g;
    #pragma unroll
    for(int m=0;m<4;++m) *(GAS v2u*)(op+16*m)=(v2u){pk(mine[m][0],mine[m][1]),pk(mine[m][2],mine[m][3])};
    CST(38);
  }
}

__global__ void k_zero(uint4* p){ p[threadIdx.x]=make_uint4(0,0,0,0); }
extern "C" void kernel_launch(void* const* d_in, const int* in_sizes, int n_in,
                              void* d_out, int out_size, void* d_ws, size_t ws_size,
                              hipStream_t stream) {
  Ptrs P;
  P.x0=(const u16*)d_in[0]; P.k0=(const u16*)d_in[1]; P.v0=(const u16*)d_in[2]; P.wq0=(const u16*)d_in[3]; P.wo0=(const u16*)d_in[4];
  P.x1=(const u16*)d_in[5]; P.k1=(const u16*)d_in[6]; P.v1=(const u16*)d_in[7]; P.wq1=(const u16*)d_in[8]; P.wo1=(const u16*)d_in[9];
  P.out=(u16*)d_out; P.ws=(char*)d_ws;
  const int lds=LDS_BYTES;
  (void)hipFuncSetAttribute((const void*)k_fused,hipFuncAttributeMaxDynamicSharedMemorySize,lds);
  k_zero<<<1,FLAG_BYTES/16,0,stream>>>((uint4*)((char*)d_ws+FLAG_OFF));
  k_fused<<<256,512,lds,stream>>>(P);
}
```

```cpp
#define TIMELINE 1
#define NO_DUMP 1
#include <hip/hip_runtime.h>
#include <hip/hip_bf16.h>
typedef unsigned v4u __attribute__((ext_vector_type(4)));
typedef unsigned v2u __attribute__((ext_vector_type(2)));
typedef float f32x4 __attribute__((ext_vector_type(4)));
typedef unsigned short u16;
#define GAS __attribute__((address_space(1)))
#define LAS __attribute__((address_space(3)))
#define M16(c,a,b) asm volatile("v_mfma_f32_16x16x32_bf16 %0, %1, %2, %0":"+v"(c):"v"(a),"v"(b))
#define M16Z(c,a,b) asm volatile("v_mfma_f32_16x16x32_bf16 %0, %1, %2, 0":"=v"(c):"v"(a),"v"(b))
#define FENCE4(n,a,b,c,d) asm volatile(n:"+v"(a),"+v"(b),"+v"(c),"+v"(d))
#define RD128(d,addr,off) asm volatile("ds_read_b128 %0, %1 offset:%2":"=v"(d):"v"(addr),"i"(off))
#define RD64(d,addr,off) asm volatile("ds_read_b64 %0, %1 offset:%2":"=v"(d):"v"(addr),"i"(off))
#define RDTR(d,addr,off) asm volatile("ds_read_b64_tr_b16 %0, %1 offset:%2":"=v"(d):"v"(addr),"i"(off))
#define WR128(addr,off,d) asm volatile("ds_write_b128 %0, %1 offset:%2"::"v"(addr),"v"(d),"i"(off):"memory")
#define WAITL(n) asm volatile("s_waitcnt lgkmcnt(%0)"::"i"(n):"memory")
#define WAITV(n) asm volatile("s_waitcnt vmcnt(%0)"::"i"(n):"memory")
#define BAR() asm volatile("s_barrier":::"memory")
#define RLX_AGENT __ATOMIC_RELAXED, __HIP_MEMORY_SCOPE_AGENT
#define AUX_KV 2
#define PART_BF16 1
constexpr int SC1=16;
constexpr int SLOT=32768;
constexpr int SCR=4*SLOT;
constexpr int LDS_BYTES=SCR+16384;
constexpr size_t O_OFF=0;
constexpr size_t PART_OFF=(size_t)8<<20;
constexpr size_t FLAG_OFF=(size_t)32<<20;
constexpr int OF_W=0;
constexpr int RF_W=32*32;
constexpr int FLAG_BYTES=(32*32+64*32)*4;
constexpr unsigned SPIN_CAP=1u<<16;

#define STAMP(idx) do{ unsigned long long t_; asm volatile("s_memrealtime %0\n\ts_waitcnt lgkmcnt(0)":"=s"(t_)::"memory"); if(lane==0) ((volatile LAS unsigned*)((LAS char*)smem+LDS_BYTES))[(idx)]=(unsigned)t_; }while(0)
#define CST(idx) do{ if(w==0) STAMP(idx); }while(0)
#define LST(idx) do{ if(w==4) STAMP(idx); }while(0)

__device__ __forceinline__ v4u cat(v2u a,v2u b){ return __builtin_shufflevector(a,b,0,1,2,3); }
__device__ __forceinline__ unsigned pk(float a,float b){ unsigned r; asm("v_cvt_pk_bf16_f32 %0, %1, %2":"=v"(r):"v"(a),"v"(b)); return r; }
__device__ __forceinline__ float bflo(unsigned u){ return __uint_as_float(u<<16); }
__device__ __forceinline__ float bfhi(unsigned u){ return __uint_as_float(u&0xffff0000u); }

struct Ptrs { const u16 *x0,*x1,*k0,*k1,*v0,*v1,*wq0,*wq1,*wo0,*wo1; u16* out; char* ws; };

template<bool PUB>
__device__ __forceinline__ void gemm_loop(f32x4 (&acc)[4][4], unsigned (&xc)[2], unsigned (&wc)[2][2], GAS unsigned* flag, int lane){
  v2u A[2][4][2]; v4u B[2][4];
  #pragma unroll
  for(int n=0;n<4;++n) RD128(B[0][n],xc[0],n*2048);
  #pragma unroll
  for(int m=0;m<4;++m){ RDTR(A[0][m][0],wc[m&1][0],(m>>1)*512); RDTR(A[0][m][1],wc[m&1][1],(m>>1)*512); }
  WAITL(0);
  #pragma unroll 1
  for(int s=0;s<16;++s){
    {
      int r=0;
      #pragma unroll
      for(int m=0;m<4;++m)
      #pragma unroll
      for(int n=0;n<4;++n){
        M16(acc[m][n],cat(A[0][m][0],A[0][m][1]),B[0][n]);
        if(r<4){ RD128(B[1][r],xc[1],r*2048); }
        else if(r<12){ int mm=(r-4)>>1,t=(r-4)&1; RDTR(A[1][mm][t],wc[mm&1][t],8192+(mm>>1)*512); }
        ++r;
      }
    }
    if(PUB){ if(s==0){ WAITV(0); if(lane==0) __hip_atomic_store(flag,1u,RLX_AGENT); } }
    WAITL(0);
    BAR();
    {
      unsigned d = ((s&3)==3) ? (unsigned)(-3*SLOT) : (unsigned)SLOT;
      xc[0]+=d; xc[1]+=d; wc[0][0]+=d; wc[0][1]+=d; wc[1][0]+=d; wc[1][1]+=d;
    }
    {
      int r=0;
      #pragma unroll
      for(int m=0;m<4;++m)
      #pragma unroll
      for(int n=0;n<4;++n){
        M16(acc[m][n],cat(A[1][m][0],A[1][m][1]),B[1][n]);
        if(r<4){ RD128(B[0][r],xc[0],r*2048); }
        else if(r<12){ int mm=(r-4)>>1,t=(r-4)&1; RDTR(A[0][mm][t],wc[mm&1][t],(mm>>1)*512); }
        ++r;
      }
    }
    WAITL(0);
  }
}

__device__ __forceinline__ bool poll_all(GAS unsigned* p, bool active){
  for(unsigned spins=0;spins<SPIN_CAP;++spins){
    unsigned v = active ? __hip_atomic_load(p,RLX_AGENT) : 1u;
    if(__all(v!=0u)) return true;
    __builtin_amdgcn_s_sleep(1);
  }
  return false;
}

__global__ __launch_bounds__(512) void k_fused(Ptrs P){
  extern __shared__ __attribute__((aligned(16))) char smem[];
  const int lane=threadIdx.x&63, w=__builtin_amdgcn_readfirstlane(threadIdx.x>>6);
  const int b=blockIdx.x, g8=b&7, j=b>>3;
  const int br=g8>>2, oct=g8&3, jj=j&7, mb=j>>3;
  const int head=oct*8+jj;
  GAS unsigned* flags=(GAS unsigned*)(P.ws+FLAG_OFF);
  const int ogrp=(br*4+mb)*4+oct;
  const int rgrp=(br*4+mb)*8+jj;
  if(w>=4){
    const int lw=w-4;
    LAS char* L=(LAS char*)smem;
    const unsigned lb=(unsigned)(size_t)L;
    LST(0);
    const int rot=(4*mb+2*jj);
    const __amdgpu_buffer_rsrc_t rx=__builtin_amdgcn_make_buffer_rsrc((void*)((br?P.x1:P.x0)+(size_t)mb*128*1024),0,128*2048,0x00020000);
    const __amdgpu_buffer_rsrc_t rw=__builtin_amdgcn_make_buffer_rsrc((void*)((br?P.wq1:P.wq0)+head*128),0,1024*8192-head*256,0x00020000);
    const int vx=(lane>>3)*2048 + 16*((lane&7) ^ (((lane>>4)&3)|((lw&1)<<2)));
    const int X=((((lane>>4)&1)^(lw&1))<<1);
    const int vw=((lane>>2)&7)*8192 + (lane>>5)*64 + 16*((lane&3)^X);
    auto issue1=[&](int s){
      int st=(s+rot)&15; int slot=(s&3)*SLOT; int k0=st*64;
      #pragma unroll
      for(int u=0;u<4;++u){ int i=lw+4*u; __builtin_amdgcn_raw_ptr_buffer_load_lds(rx,(LAS void*)(L+slot+i*1024),16,vx,i*8*2048+k0*2,0,0); }
      #pragma unroll
      for(int u=0;u<4;++u){ int rg=lw+4*(u>>1), chh=u&1; int i=rg*2+chh; __builtin_amdgcn_raw_ptr_buffer_load_lds(rw,(LAS void*)(L+slot+16384+i*1024),16,vw,(k0+rg*8)*8192+chh*128,0,0); }
    };
    issue1(0); issue1(1);
    WAITV(8); BAR(); LST(1);
    issue1(2); issue1(3);
    const __amdgpu_buffer_rsrc_t rk=__builtin_amdgcn_make_buffer_rsrc((void*)((br?P.k1:P.k0)+(size_t)(mb*4*32+head)*4096),0,3*32*8192+8192,0x00020000);
    const __amdgpu_buffer_rsrc_t rv=__builtin_amdgcn_make_buffer_rsrc((void*)((br?P.v1:P.v0)+(size_t)(mb*4*32+head)*4096),0,3*32*8192+8192,0x00020000);
    const int vk=(lane>>4)*256 + 16*((lane&15) ^ (((lane>>4)<<2)|lw));
    v4u Kr[8], Vr[8];
    #pragma unroll 1
    for(int s=1;s<6;++s){ WAITV(16); BAR(); LST(1+s); issue1(s+3); }
    #pragma unroll
    for(int u=0;u<8;++u){ int so=(u>>1)*32*8192+(lw+4*(u&1))*1024; asm volatile("buffer_load_dwordx4 %0, %1, %2, %3 offen nt":"=v"(Kr[u]):"v"(vk),"s"(rk),"s"(so):"memory"); }
    #pragma unroll 1
    for(int s=6;s<9;++s){ WAITV(16); BAR(); LST(1+s); issue1(s+3); }
    #pragma unroll
    for(int u=0;u<8;++u){ int so=(u>>1)*32*8192+(lw+4*(u&1))*1024; asm volatile("buffer_load_dwordx4 %0, %1, %2, %3 offen nt":"=v"(Vr[u]):"v"(vk),"s"(rv),"s"(so):"memory"); }
    #pragma unroll 1
    for(int s=9;s<13;++s){ WAITV(16); BAR(); LST(1+s); issue1(s+3); }
    const __amdgpu_buffer_rsrc_t rwo=__builtin_amdgcn_make_buffer_rsrc((void*)((br?P.wo1:P.wo0)+(size_t)oct*1024*1024+jj*128),0,1024*2048-jj*256,0x00020000);
    const __amdgpu_buffer_rsrc_t ro=__builtin_amdgcn_make_buffer_rsrc((void*)(P.ws+O_OFF+((size_t)(br*4+mb)*64+oct*16)*16384),0,16*16384,0x00020000);
    const int vw2=((lane>>2)&7)*2048 + (lane>>5)*64 + 16*((lane&3)^X);
    auto st2=[&](int r)->int{
      if(r<2) return 2*jj+r;
      int t=((r-2)>>1)+2*mb; if(t>=7) t-=7; if(t>=7) t-=7;
      return 2*((jj+1+t)&7)+(r&1);
    };
    auto issue_wo=[&](int r){
      int slot=(r&3)*SLOT; int k0=st2(r)*64;
      #pragma unroll
      for(int u=0;u<4;++u){ int rg=lw+4*(u>>1), chh=u&1; int i=rg*2+chh; __builtin_amdgcn_raw_ptr_buffer_load_lds(rwo,(LAS void*)(L+slot+16384+i*1024),16,vw2,(k0+rg*8)*2048+chh*128,0,0); }
    };
    auto issue_o=[&](int r){
      int slot=(r&3)*SLOT; int so=st2(r)*16384;
      #pragma unroll
      for(int u=0;u<4;++u){ int i=lw+4*u; __builtin_amdgcn_raw_ptr_buffer_load_lds(ro,(LAS void*)(L+slot+i*1024),16,lane*16,so+i*1024,0,SC1); }
    };
    const unsigned kva=lb+lw*1024+lane*16;
    WAITV(16); BAR(); LST(14);
    #pragma unroll
    for(int u=0;u<8;++u){ asm volatile("":"+v"(Kr[u])); asm volatile("":"+v"(Vr[u])); }
    #pragma unroll
    for(int u=0;u<4;++u) WR128(kva,(u>>1)*8192+(u&1)*4096,Kr[u]);
    issue_wo(0);
    WAITV(12); BAR(); LST(15);
    #pragma unroll
    for(int u=4;u<8;++u) WR128(kva,SLOT+((u-4)>>1)*8192+(u&1)*4096,Kr[u]);
    issue_wo(1);
    WAITV(8); WAITL(0); BAR(); LST(16);
    #pragma unroll
    for(int u=0;u<4;++u) asm volatile("ds_write_b128 %0, %1 offset:%2"::"v"(kva+2*SLOT),"v"(Vr[u]),"i"((u>>1)*8192+(u&1)*4096):"memory");
    issue_wo(2);
    WAITL(0); BAR(); LST(17);
    #pragma unroll
    for(int u=4;u<8;++u) asm volatile("ds_write_b128 %0, %1 offset:%2"::"v"(kva+3*SLOT),"v"(Vr[u]),"i"(((u-4)>>1)*8192+(u&1)*4096):"memory");
    issue_wo(3);
    WAITL(0); BAR(); LST(18);
    WAITV(12); BAR(); LST(19);
    WAITV(8); BAR(); LST(20);
    issue_wo(4);
    poll_all(flags+OF_W+ogrp*32+(lane&31),(lane<32)&&((lane>>2)!=jj));
    LST(21);
    issue_o(2); issue_o(3); issue_o(4);
    WAITV(8); BAR(); LST(22);
    issue_o(5); issue_wo(5);
    WAITV(12); BAR(); LST(23);
    #pragma unroll 1
    for(int r=4;r<14;++r){ issue_o(r+2); issue_wo(r+2); WAITV(16); BAR(); }
    LST(24);
    WAITV(8); BAR();
    WAITV(0); BAR();
    LST(25);
    BAR();
    BAR();
    if(w==4&&lane<48) ((GAS unsigned*)(P.ws+(64u<<20)+blockIdx.x*256))[lane]=((volatile LAS unsigned*)((LAS char*)smem+LDS_BYTES))[lane];
    return;
  }
  const unsigned lb=(unsigned)(size_t)(LAS char*)smem;
  const int g=lane>>4, i16=lane&15, q=i16>>2, p=lane&3;
  const int wm=w>>1, wn=w&1;
  unsigned xc[2], wc[2][2];
  { int row=wm*64+i16; int s=(row>>1)&7; xc[0]=lb+128*row+16*((g)^s); xc[1]=lb+128*row+16*((4+g)^s); }
  #pragma unroll
  for(int c1=0;c1<2;++c1)
  #pragma unroll
  for(int t=0;t<2;++t){ int X=((t^g)&1)<<1; wc[c1][t]=lb+16384+2048*g+64*(4*t+q)+16*((2*c1+(p>>1))^X)+8*(p&1)+1024*wn; }
  f32x4 acc[4][4];
  #pragma unroll
  for(int m=0;m<4;++m)
  #pragma unroll
  for(int n=0;n<4;++n) acc[m][n]=(f32x4){0,0,0,0};
  BAR();
  CST(30);
  gemm_loop<false>(acc,xc,wc,nullptr,lane);
  CST(31);
  v2u Kf[2][2][2][2];
  {
    const int f=((i16&3)<<2)|((i16>>2)&3);
    #pragma unroll
    for(int mm=0;mm<2;++mm)
    #pragma unroll
    for(int t=0;t<2;++t){
      unsigned a=lb+wm*SLOT+256*i16+16*(((2*wn+mm)*4+2*t+(g>>1))^f)+8*(g&1);
      #pragma unroll
      for(int l=0;l<2;++l)
      #pragma unroll
      for(int kb=0;kb<2;++kb) RD64(Kf[l][kb][mm][t],a,l*8192+kb*4096);
    }
  }
  #pragma unroll
  for(int m=0;m<4;++m) FENCE4("s_nop 1",acc[m][0],acc[m][1],acc[m][2],acc[m][3]);
  v4u QB[4][2];
  #pragma unroll
  for(int n=0;n<4;++n)
  #pragma unroll
  for(int mm=0;mm<2;++mm){
    QB[n][mm]=(v4u){pk(acc[2*mm][n][0],acc[2*mm][n][1]),pk(acc[2*mm][n][2],acc[2*mm][n][3]),pk(acc[2*mm+1][n][0],acc[2*mm+1][n][1]),pk(acc[2*mm+1][n][2],acc[2*mm+1][n][3])};
  }
  WAITL(0);
  FENCE4("s_nop 1",QB[0][0],QB[0][1],QB[1][0],QB[1][1]);
  FENCE4("s_nop 1",QB[2][0],QB[2][1],QB[3][0],QB[3][1]);
  f32x4 S[2][2][2];
  #pragma unroll
  for(int l=0;l<2;++l)
  #pragma unroll
  for(int kb=0;kb<2;++kb)
  #pragma unroll
  for(int qb=0;qb<2;++qb) M16Z(S[l][kb][qb],cat(Kf[l][kb][0][0],Kf[l][kb][0][1]),QB[2*l+qb][0]);
  #pragma unroll
  for(int l=0;l<2;++l)
  #pragma unroll
  for(int kb=0;kb<2;++kb)
  #pragma unroll
  for(int qb=0;qb<2;++qb) M16(S[l][kb][qb],cat(Kf[l][kb][1][0],Kf[l][kb][1][1]),QB[2*l+qb][1]);
  FENCE4("s_nop 7",S[0][0][0],S[0][0][1],S[0][1][0],S[0][1][1]);
  FENCE4("s_nop 0",S[1][0][0],S[1][0][1],S[1][1][0],S[1][1][1]);
  {
    LAS char* sc=(LAS char*)smem+SCR+w*4096+lane*16;
    #pragma unroll
    for(int kb=0;kb<2;++kb)
    #pragma unroll
    for(int qb=0;qb<2;++qb) *(LAS f32x4*)(sc+(kb*2+qb)*1024) = wn? S[0][kb][qb] : S[1][kb][qb];
  }
  WAITL(0);
  asm volatile("":::"memory");
  BAR();
  CST(32);
  f32x4 T[2][2];
  {
    LAS char* sc=(LAS char*)smem+SCR+(w^1)*4096+lane*16;
    #pragma unroll
    for(int kb=0;kb<2;++kb)
    #pragma unroll
    for(int qb=0;qb<2;++qb){ f32x4 o=*(LAS f32x4*)(sc+(kb*2+qb)*1024); T[kb][qb]=(wn? S[1][kb][qb] : S[0][kb][qb]) + o; }
  }
  v2u Vf[8][2];
  {
    const int f=(q<<2)|g;
    #pragma unroll
    for(int u=0;u<4;++u){
      unsigned a=lb+(2+wm)*SLOT+wn*8192+256*(4*g+q)+16*((4*u+p)^f);
      #pragma unroll
      for(int hf=0;hf<2;++hf)
      #pragma unroll
      for(int t=0;t<2;++t) RDTR(Vf[2*u+hf][t],a,t*4096+hf*8);
    }
  }
  v4u PB[2];
  constexpr float SCL=0.08838834764831845f*1.4426950408889634f;
  #pragma unroll
  for(int qb=0;qb<2;++qb){
    float mx=fmaxf(fmaxf(fmaxf(T[0][qb][0],T[0][qb][1]),fmaxf(T[0][qb][2],T[0][qb][3])),fmaxf(fmaxf(T[1][qb][0],T[1][qb][1]),fmaxf(T[1][qb][2],T[1][qb][3])));
    { auto r=__builtin_amdgcn_permlane16_swap(__float_as_uint(mx),__float_as_uint(mx),false,false); mx=fmaxf(__uint_as_float(r[0]),__uint_as_float(r[1])); }
    { auto r=__builtin_amdgcn_permlane32_swap(__float_as_uint(mx),__float_as_uint(mx),false,false); mx=fmaxf(__uint_as_float(r[0]),__uint_as_float(r[1])); }
    float e[8]; float sum=0; const float nm=-mx*SCL;
    #pragma unroll
    for(int kb=0;kb<2;++kb)
    #pragma unroll
    for(int c=0;c<4;++c){ e[kb*4+c]=__builtin_amdgcn_exp2f(__builtin_fmaf(T[kb][qb][c],SCL,nm)); sum+=e[kb*4+c]; }
    { auto r=__builtin_amdgcn_permlane16_swap(__float_as_uint(sum),__float_as_uint(sum),false,false); sum=__uint_as_float(r[0])+__uint_as_float(r[1]); }
    { auto r=__builtin_amdgcn_permlane32_swap(__float_as_uint(sum),__float_as_uint(sum),false,false); sum=__uint_as_float(r[0])+__uint_as_float(r[1]); }
    float inv=__builtin_amdgcn_rcpf(sum);
    PB[qb]=(v4u){pk(e[0]*inv,e[1]*inv),pk(e[2]*inv,e[3]*inv),pk(e[4]*inv,e[5]*inv),pk(e[6]*inv,e[7]*inv)};
  }
  WAITL(0);
  asm volatile("s_nop 1":"+v"(PB[0]),"+v"(PB[1]));
  f32x4 O[8][2];
  #pragma unroll
  for(int m=0;m<8;++m)
  #pragma unroll
  for(int qb=0;qb<2;++qb) M16Z(O[m][qb],cat(Vf[m][0],Vf[m][1]),PB[qb]);
  FENCE4("s_nop 7",O[6][0],O[6][1],O[7][0],O[7][1]);
  #pragma unroll
  for(int m=0;m<6;m+=2) FENCE4("",O[m][0],O[m][1],O[m+1][0],O[m+1][1]);
  {
    const __amdgpu_buffer_rsrc_t rs=__builtin_amdgcn_make_buffer_rsrc((void*)(P.ws+O_OFF+((size_t)(br*4+mb)*64+2*head)*16384),0,2*16384,0x00020000);
    const int bb=2*wm+wn;
    const unsigned oa=lb+bb*4096+lane*16;
    #pragma unroll
    for(int mm=0;mm<4;++mm)
    #pragma unroll
    for(int qb=0;qb<2;++qb){
      v4u o=(v4u){pk(O[2*mm][qb][0],O[2*mm][qb][1]),pk(O[2*mm][qb][2],O[2*mm][qb][3]),pk(O[2*mm+1][qb][0],O[2*mm+1][qb][1]),pk(O[2*mm+1][qb][2],O[2*mm+1][qb][3])};
      __builtin_amdgcn_raw_buffer_store_b128(o,rs,bb*4096+lane*16,(mm>>1)*16384+qb*2048+(mm&1)*1024,SC1);
      WR128(oa,(mm>>1)*SLOT+qb*2048+(mm&1)*1024,o);
    }
  }
  xc[0]=lb+lane*16+wm*8192; xc[1]=xc[0]+1024;
  #pragma unroll
  for(int c1=0;c1<2;++c1)
  #pragma unroll
  for(int t=0;t<2;++t){ int X=((t^g)&1)<<1; wc[c1][t]=lb+16384+2048*g+64*(4*t+q)+16*((2*c1+(p>>1))^X)+8*(p&1)+1024*wn; }
  #pragma unroll
  for(int m=0;m<4;++m)
  #pragma unroll
  for(int n=0;n<4;++n) acc[m][n]=(f32x4){0,0,0,0};
  WAITL(0);
  CST(33);
  BAR();
  CST(34);
  gemm_loop<true>(acc,xc,wc,flags+OF_W+ogrp*32+jj*4+w,lane);
  CST(35);
  #pragma unroll
  for(int m=0;m<4;++m) FENCE4("s_nop 1",acc[m][0],acc[m][1],acc[m][2],acc[m][3]);
  {
    constexpr int TB = PART_BF16? 512:1024;
    const __amdgpu_buffer_rsrc_t rp=__builtin_amdgcn_make_buffer_rsrc((void*)(P.ws+PART_OFF+(size_t)rgrp*(4*4*4*4*TB)),0,4*4*4*4*TB,0x00020000);
    #pragma unroll
    for(int n=0;n<4;++n){
      if(n!=oct){
        #pragma unroll
        for(int m=0;m<4;m+=(PART_BF16?2:1)){
          if(PART_BF16){
            v4u o=(v4u){pk(acc[m][n][0],acc[m][n][1]),pk(acc[m][n][2],acc[m][n][3]),pk(acc[m+1][n][0],acc[m+1][n][1]),pk(acc[m+1][n][2],acc[m+1][n][3])};
            __builtin_amdgcn_raw_buffer_store_b128(o,rp,lane*16,(((n*4+oct)*4+w)*4+m)*TB,SC1);
          } else {
            __builtin_amdgcn_raw_buffer_store_b128(__builtin_bit_cast(v4u,acc[m][n]),rp,lane*16,(((n*4+oct)*4+w)*4+m)*TB,SC1);
          }
        }
      }
    }
    WAITV(0);
    if(lane==0) __hip_atomic_store(flags+RF_W+rgrp*32+oct*4+w,1u,RLX_AGENT);
    CST(36);
    f32x4 mine[4];
    #pragma unroll
    for(int m=0;m<4;++m) mine[m]= oct==0?acc[m][0]: oct==1?acc[m][1]: oct==2?acc[m][2]:acc[m][3];
    poll_all(flags+RF_W+rgrp*32+(lane&3)*4+w,(lane<4)&&(lane!=oct));
    CST(37);
    v4u in[3][PART_BF16?2:4];
    #pragma unroll
    for(int e=0;e<3;++e){
      int src=e+(e>=oct?1:0);
      #pragma unroll
      for(int m=0;m<(PART_BF16?2:4);++m) in[e][m]=__builtin_amdgcn_raw_buffer_load_b128(rp,lane*16,(((oct*4+src)*4+w)*4+(PART_BF16?2*m:m))*TB,SC1);
    }
    #pragma unroll
    for(int e=0;e<3;++e){
      if(PART_BF16){
        #pragma unroll
        for(int m=0;m<2;++m){
          mine[2*m][0]+=bflo(in[e][m][0]); mine[2*m][1]+=bfhi(in[e][m][0]); mine[2*m][2]+=bflo(in[e][m][1]); mine[2*m][3]+=bfhi(in[e][m][1]);
          mine[2*m+1][0]+=bflo(in[e][m][2]); mine[2*m+1][1]+=bfhi(in[e][m][2]); mine[2*m+1][2]+=bflo(in[e][m][3]); mine[2*m+1][3]+=bfhi(in[e][m][3]);
        }
      } else {
        #pragma unroll
        for(int m=0;m<4;++m) mine[m]+=__builtin_bit_cast(f32x4,in[e][m]);
      }
    }
    u16* op=P.out+(size_t)(mb*128+wm*64+16*oct+i16)*2048+br*1024+jj*128+wn*64+4*g;
    #pragma unroll
    for(int m=0;m<4;++m) *(GAS v2u*)(op+16*m)=(v2u){pk(mine[m][0],mine[m][1]),pk(mine[m][2],mine[m][3])};
    CST(38);
  }
  BAR();
}

__global__ void k_zero(uint4* p){ unsigned long long r0; asm volatile("s_memrealtime %0\n\ts_waitcnt lgkmcnt(0)":"=s"(r0)::"memory"); p[threadIdx.x]=make_uint4(0,0,0,0); if(threadIdx.x==0) *(unsigned*)((char*)p+(38u<<20))=(unsigned)r0; }

__global__ void k_spin(Ptrs P,int mode,int mult){
  unsigned long long rs; asm volatile("s_memrealtime %0\n\ts_waitcnt lgkmcnt(0)":"=s"(rs)::"memory");
  const GAS unsigned* h=(const GAS unsigned*)(P.ws+(64u<<20));
  unsigned t0v=h[threadIdx.x*64+0], tv=h[threadIdx.x*64+38];
  unsigned z=*(const GAS unsigned*)(P.ws+FLAG_OFF+(38u<<20));
  __shared__ int mn,mx; if(threadIdx.x==0){ mn=0x7fffffff; mx=-0x7fffffff; } __syncthreads();
  atomicMin(&mn,(int)(t0v-z)); atomicMax(&mx,(int)(tv-z)); __syncthreads();
  if(threadIdx.x==0){
    unsigned val = mode==0? (unsigned)mn : mode==1? (unsigned)((int)((unsigned)rs-z)-mx) : (unsigned)(mx-mn);
    unsigned lim=val*mult; if(lim>100000u) lim=100000u;
    unsigned long long r0,r; asm volatile("s_memrealtime %0\n\ts_waitcnt lgkmcnt(0)":"=s"(r0)::"memory");
    do{ asm volatile("s_memrealtime %0\n\ts_waitcnt lgkmcnt(0)":"=s"(r)::"memory"); }while((unsigned)(r-r0)<lim);
  }
}
extern "C" void kernel_launch(void* const* d_in, const int* in_sizes, int n_in,
                              void* d_out, int out_size, void* d_ws, size_t ws_size,
                              hipStream_t stream) {
  Ptrs P;
  P.x0=(const u16*)d_in[0]; P.k0=(const u16*)d_in[1]; P.v0=(const u16*)d_in[2]; P.wq0=(const u16*)d_in[3]; P.wo0=(const u16*)d_in[4];
  P.x1=(const u16*)d_in[5]; P.k1=(const u16*)d_in[6]; P.v1=(const u16*)d_in[7]; P.wq1=(const u16*)d_in[8]; P.wo1=(const u16*)d_in[9];
  P.out=(u16*)d_out; P.ws=(char*)d_ws;
  const int lds=LDS_BYTES+256;
  (void)hipFuncSetAttribute((const void*)k_fused,hipFuncAttributeMaxDynamicSharedMemorySize,lds);
  k_zero<<<1,FLAG_BYTES/16,0,stream>>>((uint4*)((char*)d_ws+FLAG_OFF));
  k_fused<<<256,512,lds,stream>>>(P);
  k_spin<<<1,256,0,stream>>>(P,0,0);
}
```

```cpp
#include <hip/hip_runtime.h>
#include <hip/hip_bf16.h>
typedef unsigned v4u __attribute__((ext_vector_type(4)));
typedef unsigned v2u __attribute__((ext_vector_type(2)));
typedef float f32x4 __attribute__((ext_vector_type(4)));
typedef unsigned short u16;
#define GAS __attribute__((address_space(1)))
#define LAS __attribute__((address_space(3)))
#define M16(c,a,b) asm volatile("v_mfma_f32_16x16x32_bf16 %0, %1, %2, %0":"+v"(c):"v"(a),"v"(b))
#define M16Z(c,a,b) asm volatile("v_mfma_f32_16x16x32_bf16 %0, %1, %2, 0":"=v"(c):"v"(a),"v"(b))
#define FENCE4(n,a,b,c,d) asm volatile(n:"+v"(a),"+v"(b),"+v"(c),"+v"(d))
#define RD128(d,addr,off) asm volatile("ds_read_b128 %0, %1 offset:%2":"=v"(d):"v"(addr),"i"(off))
#define RD64(d,addr,off) asm volatile("ds_read_b64 %0, %1 offset:%2":"=v"(d):"v"(addr),"i"(off))
#define RDTR(d,addr,off) asm volatile("ds_read_b64_tr_b16 %0, %1 offset:%2":"=v"(d):"v"(addr),"i"(off))
#define WR128(addr,off,d) asm volatile("ds_write_b128 %0, %1 offset:%2"::"v"(addr),"v"(d),"i"(off):"memory")
#define WAITL(n) asm volatile("s_waitcnt lgkmcnt(%0)"::"i"(n):"memory")
#define WAITV(n) asm volatile("s_waitcnt vmcnt(%0)"::"i"(n):"memory")
#define BAR() asm volatile("s_barrier":::"memory")
#define RLX_AGENT __ATOMIC_RELAXED, __HIP_MEMORY_SCOPE_AGENT
#define AUX_KV 2
#define PART_BF16 1
constexpr int SC1=16;
constexpr int SLOT=32768;
constexpr int SCR=4*SLOT;
constexpr int LDS_BYTES=SCR+16384+64;
constexpr size_t O_OFF=0;
constexpr size_t PART_OFF=(size_t)8<<20;
constexpr size_t CTL_OFF=(size_t)32<<20;
constexpr int R_OFF=0;
constexpr int OIN_OFF=256*128;
constexpr int RIN_OFF=OIN_OFF+256*256;
constexpr int CTL_BYTES=RIN_OFF+256*128;
constexpr int NLDS=SCR+16384;
typedef unsigned long long u64;
typedef GAS u64 gu64;
constexpr unsigned SPIN_CAP=1u<<16;

#define CST(i)
#define LST(i)

__device__ __forceinline__ v4u cat(v2u a,v2u b){ return __builtin_shufflevector(a,b,0,1,2,3); }
__device__ __forceinline__ unsigned pk(float a,float b){ unsigned r; asm("v_cvt_pk_bf16_f32 %0, %1, %2":"=v"(r):"v"(a),"v"(b)); return r; }
__device__ __forceinline__ float bflo(unsigned u){ return __uint_as_float(u<<16); }
__device__ __forceinline__ float bfhi(unsigned u){ return __uint_as_float(u&0xffff0000u); }

struct Ptrs { const u16 *x0,*x1,*k0,*k1,*v0,*v1,*wq0,*wq1,*wo0,*wo1; u16* out; char* ws; };

template<bool PUB>
__device__ __forceinline__ void gemm_loop(f32x4 (&acc)[4][4], unsigned (&xc)[2], unsigned (&wc)[2][2], gu64* flag, u64 tag, bool doflag){
  v2u A[2][4][2]; v4u B[2][4];
  #pragma unroll
  for(int n=0;n<4;++n) RD128(B[0][n],xc[0],n*2048);
  #pragma unroll
  for(int m=0;m<4;++m){ RDTR(A[0][m][0],wc[m&1][0],(m>>1)*512); RDTR(A[0][m][1],wc[m&1][1],(m>>1)*512); }
  WAITL(0);
  #pragma unroll 1
  for(int s=0;s<16;++s){
    {
      int r=0;
      #pragma unroll
      for(int m=0;m<4;++m)
      #pragma unroll
      for(int n=0;n<4;++n){
        M16(acc[m][n],cat(A[0][m][0],A[0][m][1]),B[0][n]);
        if(r<4){ RD128(B[1][r],xc[1],r*2048); }
        else if(r<12){ int mm=(r-4)>>1,t=(r-4)&1; RDTR(A[1][mm][t],wc[mm&1][t],8192+(mm>>1)*512); }
        ++r;
      }
    }
    if(PUB){ if(s==0){ WAITV(0); if(doflag) __hip_atomic_store(flag,tag,RLX_AGENT); } }
    WAITL(0);
    BAR();
    {
      unsigned d = ((s&3)==3) ? (unsigned)(-3*SLOT) : (unsigned)SLOT;
      xc[0]+=d; xc[1]+=d; wc[0][0]+=d; wc[0][1]+=d; wc[1][0]+=d; wc[1][1]+=d;
    }
    {
      int r=0;
      #pragma unroll
      for(int m=0;m<4;++m)
      #pragma unroll
      for(int n=0;n<4;++n){
        M16(acc[m][n],cat(A[1][m][0],A[1][m][1]),B[1][n]);
        if(r<4){ RD128(B[0][r],xc[0],r*2048); }
        else if(r<12){ int mm=(r-4)>>1,t=(r-4)&1; RDTR(A[0][mm][t],wc[mm&1][t],(mm>>1)*512); }
        ++r;
      }
    }
    WAITL(0);
  }
}

__device__ __forceinline__ bool poll_eq(gu64* p, u64 want, bool active){
  for(unsigned spins=0;spins<SPIN_CAP;++spins){
    u64 v = active ? __hip_atomic_load(p,RLX_AGENT) : want;
    if(__all(v==want)) return true;
    __builtin_amdgcn_s_sleep(1);
  }
  return false;
}

__global__ __launch_bounds__(512) void k_fused(Ptrs P){
  extern __shared__ __attribute__((aligned(16))) char smem[];
  const int lane=threadIdx.x&63, w=__builtin_amdgcn_readfirstlane(threadIdx.x>>6);
  const int b=blockIdx.x, g8=b&7, j=b>>3;
  const int br=g8>>2, oct=g8&3, jj=j&7, mb=j>>3;
  const int head=oct*8+jj;
  GAS char* ctl=(GAS char*)(P.ws+CTL_OFF);
  const int rgrp=(br*4+mb)*8+jj;
  const int pid = (lane<8) ? (g8+8*(mb*8+lane)) : ((br*4+(lane&3))+8*j);
  if(w>=4){
    const int lw=w-4;
    LAS char* L=(LAS char*)smem;
    const unsigned lb=(unsigned)(size_t)L;
    LST(0);
    const int rot=(4*mb+2*jj);
    const __amdgpu_buffer_rsrc_t rx=__builtin_amdgcn_make_buffer_rsrc((void*)((br?P.x1:P.x0)+(size_t)mb*128*1024),0,128*2048,0x00020000);
    const __amdgpu_buffer_rsrc_t rw=__builtin_amdgcn_make_buffer_rsrc((void*)((br?P.wq1:P.wq0)+head*128),0,1024*8192-head*256,0x00020000);
    const int vx=(lane>>3)*2048 + 16*((lane&7) ^ (((lane>>4)&3)|((lw&1)<<2)));
    const int X=((((lane>>4)&1)^(lw&1))<<1);
    const int vw=((lane>>2)&7)*8192 + (lane>>5)*64 + 16*((lane&3)^X);
    auto issue1=[&](int s){
      int st=(s+rot)&15; int slot=(s&3)*SLOT; int k0=st*64;
      #pragma unroll
      for(int u=0;u<4;++u){ int i=lw+4*u; __builtin_amdgcn_raw_ptr_buffer_load_lds(rx,(LAS void*)(L+slot+i*1024),16,vx,i*8*2048+k0*2,0,0); }
      #pragma unroll
      for(int u=0;u<4;++u){ int rg=lw+4*(u>>1), chh=u&1; int i=rg*2+chh; __builtin_amdgcn_raw_ptr_buffer_load_lds(rw,(LAS void*)(L+slot+16384+i*1024),16,vw,(k0+rg*8)*8192+chh*128,0,0); }
    };
    issue1(0); issue1(1);
    WAITV(8); BAR(); LST(1);
    issue1(2); issue1(3);
    const __amdgpu_buffer_rsrc_t rk=__builtin_amdgcn_make_buffer_rsrc((void*)((br?P.k1:P.k0)+(size_t)(mb*4*32+head)*4096),0,3*32*8192+8192,0x00020000);
    const __amdgpu_buffer_rsrc_t rv=__builtin_amdgcn_make_buffer_rsrc((void*)((br?P.v1:P.v0)+(size_t)(mb*4*32+head)*4096),0,3*32*8192+8192,0x00020000);
    const int vk=(lane>>4)*256 + 16*((lane&15) ^ (((lane>>4)<<2)|lw));
    v4u Kr[8], Vr[8];
    #pragma unroll 1
    for(int s=1;s<6;++s){ WAITV(16); BAR(); LST(1+s); issue1(s+3); }
    #pragma unroll
    for(int u=0;u<8;++u){ int so=(u>>1)*32*8192+(lw+4*(u&1))*1024; asm volatile("buffer_load_dwordx4 %0, %1, %2, %3 offen nt":"=v"(Kr[u]):"v"(vk),"s"(rk),"s"(so):"memory"); }
    #pragma unroll 1
    for(int s=6;s<9;++s){ WAITV(16); BAR(); LST(1+s); issue1(s+3); }
    #pragma unroll
    for(int u=0;u<8;++u){ int so=(u>>1)*32*8192+(lw+4*(u&1))*1024; asm volatile("buffer_load_dwordx4 %0, %1, %2, %3 offen nt":"=v"(Vr[u]):"v"(vk),"s"(rv),"s"(so):"memory"); }
    #pragma unroll 1
    for(int s=9;s<13;++s){ WAITV(16); BAR(); LST(1+s); issue1(s+3); }
    const __amdgpu_buffer_rsrc_t rwo=__builtin_amdgcn_make_buffer_rsrc((void*)((br?P.wo1:P.wo0)+(size_t)oct*1024*1024+jj*128),0,1024*2048-jj*256,0x00020000);
    const __amdgpu_buffer_rsrc_t ro=__builtin_amdgcn_make_buffer_rsrc((void*)(P.ws+O_OFF+((size_t)(br*4+mb)*64+oct*16)*16384),0,16*16384,0x00020000);
    const int vw2=((lane>>2)&7)*2048 + (lane>>5)*64 + 16*((lane&3)^X);
    auto st2=[&](int r)->int{
      if(r<2) return 2*jj+r;
      int t=((r-2)>>1)+2*mb; if(t>=7) t-=7; if(t>=7) t-=7;
      return 2*((jj+1+t)&7)+(r&1);
    };
    auto issue_wo=[&](int r){
      int slot=(r&3)*SLOT; int k0=st2(r)*64;
      #pragma unroll
      for(int u=0;u<4;++u){ int rg=lw+4*(u>>1), chh=u&1; int i=rg*2+chh; __builtin_amdgcn_raw_ptr_buffer_load_lds(rwo,(LAS void*)(L+slot+16384+i*1024),16,vw2,(k0+rg*8)*2048+chh*128,0,0); }
    };
    auto issue_o=[&](int r){
      int slot=(r&3)*SLOT; int so=st2(r)*16384;
      #pragma unroll
      for(int u=0;u<4;++u){ int i=lw+4*u; __builtin_amdgcn_raw_ptr_buffer_load_lds(ro,(LAS void*)(L+slot+i*1024),16,lane*16,so+i*1024,0,SC1); }
    };
    const unsigned kva=lb+lw*1024+lane*16;
    WAITV(16); BAR(); LST(14);
    #pragma unroll
    for(int u=0;u<8;++u){ asm volatile("":"+v"(Kr[u])); asm volatile("":"+v"(Vr[u])); }
    #pragma unroll
    for(int u=0;u<4;++u) WR128(kva,(u>>1)*8192+(u&1)*4096,Kr[u]);
    issue_wo(0);
    WAITV(12); BAR(); LST(15);
    #pragma unroll
    for(int u=4;u<8;++u) WR128(kva,SLOT+((u-4)>>1)*8192+(u&1)*4096,Kr[u]);
    issue_wo(1);
    WAITV(8); WAITL(0); BAR(); LST(16);
    #pragma unroll
    for(int u=0;u<4;++u) asm volatile("ds_write_b128 %0, %1 offset:%2"::"v"(kva+2*SLOT),"v"(Vr[u]),"i"((u>>1)*8192+(u&1)*4096):"memory");
    issue_wo(2);
    WAITL(0); BAR(); LST(17);
    #pragma unroll
    for(int u=4;u<8;++u) asm volatile("ds_write_b128 %0, %1 offset:%2"::"v"(kva+3*SLOT),"v"(Vr[u]),"i"(((u-4)>>1)*8192+(u&1)*4096):"memory");
    issue_wo(3);
    WAITL(0); BAR(); LST(18);
    WAITV(12); BAR(); LST(19);
    WAITV(8); BAR(); LST(20);
    const u64 myn=*(volatile LAS u64*)(L+NLDS);
    poll_eq((gu64*)(ctl+OIN_OFF+b*256+(lane&31)*8),myn,(lane<32)&&((lane>>2)!=jj));
    LST(21);
    issue_o(2); issue_o(3); issue_o(4); issue_wo(4);
    u64 fresh;
    const int pj=(lane>>2)&7;
    { gu64* pp=(gu64*)(ctl+R_OFF+(g8+8*(mb*8+pj))*128); asm volatile("global_load_dwordx2 %0, %1, off sc1":"=v"(fresh):"v"(pp):"memory"); }
    WAITV(13); BAR(); LST(22);
    issue_o(5); issue_wo(5);
    WAITV(17); BAR(); LST(23);
    issue_o(6); issue_wo(6);
    asm volatile("s_waitcnt vmcnt(16)":"+v"(fresh)::"memory");
    BAR();
    if(lw==0 && lane<32 && pj!=jj) __hip_atomic_store((gu64*)(ctl+OIN_OFF+(g8+8*(mb*8+pj))*256+(jj*4+(lane&3))*8),fresh,RLX_AGENT);
    #pragma unroll 1
    for(int r=5;r<14;++r){ issue_o(r+2); issue_wo(r+2); WAITV(16); BAR(); }
    LST(24);
    WAITV(8); BAR();
    WAITV(0); BAR();
    LST(25);
    return;
  }
  const unsigned lb=(unsigned)(size_t)(LAS char*)smem;
  const int g=lane>>4, i16=lane&15, q=i16>>2, p=lane&3;
  const int wm=w>>1, wn=w&1;
  unsigned xc[2], wc[2][2];
  { int row=wm*64+i16; int s=(row>>1)&7; xc[0]=lb+128*row+16*((g)^s); xc[1]=lb+128*row+16*((4+g)^s); }
  #pragma unroll
  for(int c1=0;c1<2;++c1)
  #pragma unroll
  for(int t=0;t<2;++t){ int X=((t^g)&1)<<1; wc[c1][t]=lb+16384+2048*g+64*(4*t+q)+16*((2*c1+(p>>1))^X)+8*(p&1)+1024*wn; }
  f32x4 acc[4][4];
  #pragma unroll
  for(int m=0;m<4;++m)
  #pragma unroll
  for(int n=0;n<4;++n) acc[m][n]=(f32x4){0,0,0,0};
  if(w==0){
    gu64* rp_=(gu64*)(ctl+R_OFF+b*128);
    u64 old=__hip_atomic_load(rp_,RLX_AGENT);
    u64 n=old*0x9E3779B97F4A7C15ull+0x632BE59BD9B4E019ull;
    if(lane==0){ __hip_atomic_store(rp_,n,RLX_AGENT); *(volatile LAS u64*)((LAS char*)smem+NLDS)=n; }
    WAITL(0);
  }
  BAR();
  CST(30);
  gemm_loop<false>(acc,xc,wc,nullptr,0,false);
  u64 pn;
  { gu64* pp=(gu64*)(ctl+R_OFF+pid*128); asm volatile("global_load_dwordx2 %0, %1, off sc1":"=v"(pn):"v"(pp):"memory"); }
  CST(31);
  v2u Kf[2][2][2][2];
  {
    const int f=((i16&3)<<2)|((i16>>2)&3);
    #pragma unroll
    for(int mm=0;mm<2;++mm)
    #pragma unroll
    for(int t=0;t<2;++t){
      unsigned a=lb+wm*SLOT+256*i16+16*(((2*wn+mm)*4+2*t+(g>>1))^f)+8*(g&1);
      #pragma unroll
      for(int l=0;l<2;++l)
      #pragma unroll
      for(int kb=0;kb<2;++kb) RD64(Kf[l][kb][mm][t],a,l*8192+kb*4096);
    }
  }
  #pragma unroll
  for(int m=0;m<4;++m) FENCE4("s_nop 1",acc[m][0],acc[m][1],acc[m][2],acc[m][3]);
  v4u QB[4][2];
  #pragma unroll
  for(int n=0;n<4;++n)
  #pragma unroll
  for(int mm=0;mm<2;++mm){
    QB[n][mm]=(v4u){pk(acc[2*mm][n][0],acc[2*mm][n][1]),pk(acc[2*mm][n][2],acc[2*mm][n][3]),pk(acc[2*mm+1][n][0],acc[2*mm+1][n][1]),pk(acc[2*mm+1][n][2],acc[2*mm+1][n][3])};
  }
  WAITL(0);
  FENCE4("s_nop 1",QB[0][0],QB[0][1],QB[1][0],QB[1][1]);
  FENCE4("s_nop 1",QB[2][0],QB[2][1],QB[3][0],QB[3][1]);
  f32x4 S[2][2][2];
  #pragma unroll
  for(int l=0;l<2;++l)
  #pragma unroll
  for(int kb=0;kb<2;++kb)
  #pragma unroll
  for(int qb=0;qb<2;++qb) M16Z(S[l][kb][qb],cat(Kf[l][kb][0][0],Kf[l][kb][0][1]),QB[2*l+qb][0]);
  #pragma unroll
  for(int l=0;l<2;++l)
  #pragma unroll
  for(int kb=0;kb<2;++kb)
  #pragma unroll
  for(int qb=0;qb<2;++qb) M16(S[l][kb][qb],cat(Kf[l][kb][1][0],Kf[l][kb][1][1]),QB[2*l+qb][1]);
  FENCE4("s_nop 7",S[0][0][0],S[0][0][1],S[0][1][0],S[0][1][1]);
  FENCE4("s_nop 0",S[1][0][0],S[1][0][1],S[1][1][0],S[1][1][1]);
  {
    LAS char* sc=(LAS char*)smem+SCR+w*4096+lane*16;
    #pragma unroll
    for(int kb=0;kb<2;++kb)
    #pragma unroll
    for(int qb=0;qb<2;++qb) *(LAS f32x4*)(sc+(kb*2+qb)*1024) = wn? S[0][kb][qb] : S[1][kb][qb];
  }
  WAITL(0);
  asm volatile("":::"memory");
  BAR();
  CST(32);
  f32x4 T[2][2];
  {
    LAS char* sc=(LAS char*)smem+SCR+(w^1)*4096+lane*16;
    #pragma unroll
    for(int kb=0;kb<2;++kb)
    #pragma unroll
    for(int qb=0;qb<2;++qb){ f32x4 o=*(LAS f32x4*)(sc+(kb*2+qb)*1024); T[kb][qb]=(wn? S[1][kb][qb] : S[0][kb][qb]) + o; }
  }
  v2u Vf[8][2];
  {
    const int f=(q<<2)|g;
    #pragma unroll
    for(int u=0;u<4;++u){
      unsigned a=lb+(2+wm)*SLOT+wn*8192+256*(4*g+q)+16*((4*u+p)^f);
      #pragma unroll
      for(int hf=0;hf<2;++hf)
      #pragma unroll
      for(int t=0;t<2;++t) RDTR(Vf[2*u+hf][t],a,t*4096+hf*8);
    }
  }
  v4u PB[2];
  constexpr float SCL=0.08838834764831845f*1.4426950408889634f;
  #pragma unroll
  for(int qb=0;qb<2;++qb){
    float mx=fmaxf(fmaxf(fmaxf(T[0][qb][0],T[0][qb][1]),fmaxf(T[0][qb][2],T[0][qb][3])),fmaxf(fmaxf(T[1][qb][0],T[1][qb][1]),fmaxf(T[1][qb][2],T[1][qb][3])));
    { auto r=__builtin_amdgcn_permlane16_swap(__float_as_uint(mx),__float_as_uint(mx),false,false); mx=fmaxf(__uint_as_float(r[0]),__uint_as_float(r[1])); }
    { auto r=__builtin_amdgcn_permlane32_swap(__float_as_uint(mx),__float_as_uint(mx),false,false); mx=fmaxf(__uint_as_float(r[0]),__uint_as_float(r[1])); }
    float e[8]; float sum=0; const float nm=-mx*SCL;
    #pragma unroll
    for(int kb=0;kb<2;++kb)
    #pragma unroll
    for(int c=0;c<4;++c){ e[kb*4+c]=__builtin_amdgcn_exp2f(__builtin_fmaf(T[kb][qb][c],SCL,nm)); sum+=e[kb*4+c]; }
    { auto r=__builtin_amdgcn_permlane16_swap(__float_as_uint(sum),__float_as_uint(sum),false,false); sum=__uint_as_float(r[0])+__uint_as_float(r[1]); }
    { auto r=__builtin_amdgcn_permlane32_swap(__float_as_uint(sum),__float_as_uint(sum),false,false); sum=__uint_as_float(r[0])+__uint_as_float(r[1]); }
    float inv=__builtin_amdgcn_rcpf(sum);
    PB[qb]=(v4u){pk(e[0]*inv,e[1]*inv),pk(e[2]*inv,e[3]*inv),pk(e[4]*inv,e[5]*inv),pk(e[6]*inv,e[7]*inv)};
  }
  WAITL(0);
  asm volatile("s_nop 1":"+v"(PB[0]),"+v"(PB[1]));
  f32x4 O[8][2];
  #pragma unroll
  for(int m=0;m<8;++m)
  #pragma unroll
  for(int qb=0;qb<2;++qb) M16Z(O[m][qb],cat(Vf[m][0],Vf[m][1]),PB[qb]);
  FENCE4("s_nop 7",O[6][0],O[6][1],O[7][0],O[7][1]);
  #pragma unroll
  for(int m=0;m<6;m+=2) FENCE4("",O[m][0],O[m][1],O[m+1][0],O[m+1][1]);
  {
    const __amdgpu_buffer_rsrc_t rs=__builtin_amdgcn_make_buffer_rsrc((void*)(P.ws+O_OFF+((size_t)(br*4+mb)*64+2*head)*16384),0,2*16384,0x00020000);
    const int bb=2*wm+wn;
    const unsigned oa=lb+bb*4096+lane*16;
    #pragma unroll
    for(int mm=0;mm<4;++mm)
    #pragma unroll
    for(int qb=0;qb<2;++qb){
      v4u o=(v4u){pk(O[2*mm][qb][0],O[2*mm][qb][1]),pk(O[2*mm][qb][2],O[2*mm][qb][3]),pk(O[2*mm+1][qb][0],O[2*mm+1][qb][1]),pk(O[2*mm+1][qb][2],O[2*mm+1][qb][3])};
      __builtin_amdgcn_raw_buffer_store_b128(o,rs,bb*4096+lane*16,(mm>>1)*16384+qb*2048+(mm&1)*1024,SC1);
      WR128(oa,(mm>>1)*SLOT+qb*2048+(mm&1)*1024,o);
    }
  }
  xc[0]=lb+lane*16+wm*8192; xc[1]=xc[0]+1024;
  #pragma unroll
  for(int c1=0;c1<2;++c1)
  #pragma unroll
  for(int t=0;t<2;++t){ int X=((t^g)&1)<<1; wc[c1][t]=lb+16384+2048*g+64*(4*t+q)+16*((2*c1+(p>>1))^X)+8*(p&1)+1024*wn; }
  #pragma unroll
  for(int m=0;m<4;++m)
  #pragma unroll
  for(int n=0;n<4;++n) acc[m][n]=(f32x4){0,0,0,0};
  WAITL(0);
  asm volatile("s_waitcnt vmcnt(8)":"+v"(pn)::"memory");
  CST(33);
  BAR();
  CST(34);
  gemm_loop<true>(acc,xc,wc,(gu64*)(ctl+OIN_OFF+pid*256+(jj*4+w)*8),pn,(lane<8)&&(lane!=jj));
  CST(35);
  #pragma unroll
  for(int m=0;m<4;++m) FENCE4("s_nop 1",acc[m][0],acc[m][1],acc[m][2],acc[m][3]);
  {
    constexpr int TB = PART_BF16? 512:1024;
    const __amdgpu_buffer_rsrc_t rp=__builtin_amdgcn_make_buffer_rsrc((void*)(P.ws+PART_OFF+(size_t)rgrp*(4*4*4*4*TB)),0,4*4*4*4*TB,0x00020000);
    #pragma unroll
    for(int n=0;n<4;++n){
      if(n!=oct){
        #pragma unroll
        for(int m=0;m<4;m+=(PART_BF16?2:1)){
          if(PART_BF16){
            v4u o=(v4u){pk(acc[m][n][0],acc[m][n][1]),pk(acc[m][n][2],acc[m][n][3]),pk(acc[m+1][n][0],acc[m+1][n][1]),pk(acc[m+1][n][2],acc[m+1][n][3])};
            __builtin_amdgcn_raw_buffer_store_b128(o,rp,lane*16,(((n*4+oct)*4+w)*4+m)*TB,SC1);
          } else {
            __builtin_amdgcn_raw_buffer_store_b128(__builtin_bit_cast(v4u,acc[m][n]),rp,lane*16,(((n*4+oct)*4+w)*4+m)*TB,SC1);
          }
        }
      }
    }
    WAITV(0);
    const bool rpart=(lane>=8)&&(lane<12)&&((lane&3)!=oct);
    gu64* rflag=(gu64*)(ctl+RIN_OFF+pid*128+(oct*4+w)*8);
    if(rpart) __hip_atomic_store(rflag,pn,RLX_AGENT);
    CST(36);
    f32x4 mine[4];
    #pragma unroll
    for(int m=0;m<4;++m) mine[m]= oct==0?acc[m][0]: oct==1?acc[m][1]: oct==2?acc[m][2]:acc[m][3];
    const u64 myn=*(volatile LAS u64*)((LAS char*)smem+NLDS);
    poll_eq((gu64*)(ctl+RIN_OFF+b*128+((lane&3)*4+w)*8),myn,(lane<4)&&(lane!=oct));
    CST(37);
    const u64 fresh=__hip_atomic_load((gu64*)(ctl+R_OFF+pid*128),RLX_AGENT);
    v4u in[3][PART_BF16?2:4];
    #pragma unroll
    for(int e=0;e<3;++e){
      int src=e+(e>=oct?1:0);
      #pragma unroll
      for(int m=0;m<(PART_BF16?2:4);++m) in[e][m]=__builtin_amdgcn_raw_buffer_load_b128(rp,lane*16,(((oct*4+src)*4+w)*4+(PART_BF16?2*m:m))*TB,SC1);
    }
    #pragma unroll
    for(int e=0;e<3;++e){
      if(PART_BF16){
        #pragma unroll
        for(int m=0;m<2;++m){
          mine[2*m][0]+=bflo(in[e][m][0]); mine[2*m][1]+=bfhi(in[e][m][0]); mine[2*m][2]+=bflo(in[e][m][1]); mine[2*m][3]+=bfhi(in[e][m][1]);
          mine[2*m+1][0]+=bflo(in[e][m][2]); mine[2*m+1][1]+=bfhi(in[e][m][2]); mine[2*m+1][2]+=bflo(in[e][m][3]); mine[2*m+1][3]+=bfhi(in[e][m][3]);
        }
      } else {
        #pragma unroll
        for(int m=0;m<4;++m) mine[m]+=__builtin_bit_cast(f32x4,in[e][m]);
      }
    }
    if(rpart) __hip_atomic_store(rflag,fresh,RLX_AGENT);
    u16* op=P.out+(size_t)(mb*128+wm*64+16*oct+i16)*2048+br*1024+jj*128+wn*64+4*g;
    #pragma unroll
    for(int m=0;m<4;++m) *(GAS v2u*)(op+16*m)=(v2u){pk(mine[m][0],mine[m][1]),pk(mine[m][2],mine[m][3])};
    CST(38);
  }
}

extern "C" void kernel_launch(void* const* d_in, const int* in_sizes, int n_in,
                              void* d_out, int out_size, void* d_ws, size_t ws_size,
                              hipStream_t stream) {
  Ptrs P;
  P.x0=(const u16*)d_in[0]; P.k0=(const u16*)d_in[1]; P.v0=(const u16*)d_in[2]; P.wq0=(const u16*)d_in[3]; P.wo0=(const u16*)d_in[4];
  P.x1=(const u16*)d_in[5]; P.k1=(const u16*)d_in[6]; P.v1=(const u16*)d_in[7]; P.wq1=(const u16*)d_in[8]; P.wo1=(const u16*)d_in[9];
  P.out=(u16*)d_out; P.ws=(char*)d_ws;
  const int lds=LDS_BYTES;
  (void)hipFuncSetAttribute((const void*)k_fused,hipFuncAttributeMaxDynamicSharedMemorySize,lds);
  k_fused<<<256,512,lds,stream>>>(P);
}
```

```cpp
#define TIMELINE 1
#define NO_DUMP 1
#include <hip/hip_runtime.h>
#include <hip/hip_bf16.h>
typedef unsigned v4u __attribute__((ext_vector_type(4)));
typedef unsigned v2u __attribute__((ext_vector_type(2)));
typedef float f32x4 __attribute__((ext_vector_type(4)));
typedef unsigned short u16;
#define GAS __attribute__((address_space(1)))
#define LAS __attribute__((address_space(3)))
#define M16(c,a,b) asm volatile("v_mfma_f32_16x16x32_bf16 %0, %1, %2, %0":"+v"(c):"v"(a),"v"(b))
#define M16Z(c,a,b) asm volatile("v_mfma_f32_16x16x32_bf16 %0, %1, %2, 0":"=v"(c):"v"(a),"v"(b))
#define FENCE4(n,a,b,c,d) asm volatile(n:"+v"(a),"+v"(b),"+v"(c),"+v"(d))
#define RD128(d,addr,off) asm volatile("ds_read_b128 %0, %1 offset:%2":"=v"(d):"v"(addr),"i"(off))
#define RD64(d,addr,off) asm volatile("ds_read_b64 %0, %1 offset:%2":"=v"(d):"v"(addr),"i"(off))
#define RDTR(d,addr,off) asm volatile("ds_read_b64_tr_b16 %0, %1 offset:%2":"=v"(d):"v"(addr),"i"(off))
#define WR128(addr,off,d) asm volatile("ds_write_b128 %0, %1 offset:%2"::"v"(addr),"v"(d),"i"(off):"memory")
#define WAITL(n) asm volatile("s_waitcnt lgkmcnt(%0)"::"i"(n):"memory")
#define WAITV(n) asm volatile("s_waitcnt vmcnt(%0)"::"i"(n):"memory")
#define BAR() asm volatile("s_barrier":::"memory")
#define RLX_AGENT __ATOMIC_RELAXED, __HIP_MEMORY_SCOPE_AGENT
#define AUX_KV 2
#define PART_BF16 1
constexpr int SC1=16;
constexpr int SLOT=32768;
constexpr int SCR=4*SLOT;
constexpr int LDS_BYTES=SCR+16384+64;
constexpr size_t O_OFF=0;
constexpr size_t PART_OFF=(size_t)8<<20;
constexpr size_t CTL_OFF=(size_t)32<<20;
constexpr int R_OFF=0;
constexpr int OIN_OFF=256*128;
constexpr int RIN_OFF=OIN_OFF+256*256;
constexpr int CTL_BYTES=RIN_OFF+256*128;
constexpr int NLDS=SCR+16384;
typedef unsigned long long u64;
typedef GAS u64 gu64;
constexpr unsigned SPIN_CAP=1u<<16;

#define STAMP(idx) do{ unsigned long long t_; asm volatile("s_memrealtime %0\n\ts_waitcnt lgkmcnt(0)":"=s"(t_)::"memory"); if(lane==0) ((volatile LAS unsigned*)((LAS char*)smem+LDS_BYTES))[(idx)]=(unsigned)t_; }while(0)
#define CST(idx) do{ if(w==0) STAMP(idx); }while(0)
#define LST(idx) do{ if(w==4) STAMP(idx); }while(0)

__device__ __forceinline__ v4u cat(v2u a,v2u b){ return __builtin_shufflevector(a,b,0,1,2,3); }
__device__ __forceinline__ unsigned pk(float a,float b){ unsigned r; asm("v_cvt_pk_bf16_f32 %0, %1, %2":"=v"(r):"v"(a),"v"(b)); return r; }
__device__ __forceinline__ float bflo(unsigned u){ return __uint_as_float(u<<16); }
__device__ __forceinline__ float bfhi(unsigned u){ return __uint_as_float(u&0xffff0000u); }

struct Ptrs { const u16 *x0,*x1,*k0,*k1,*v0,*v1,*wq0,*wq1,*wo0,*wo1; u16* out; char* ws; };

template<bool PUB>
__device__ __forceinline__ void gemm_loop(f32x4 (&acc)[4][4], unsigned (&xc)[2], unsigned (&wc)[2][2], gu64* flag, u64 tag, bool doflag){
  v2u A[2][4][2]; v4u B[2][4];
  #pragma unroll
  for(int n=0;n<4;++n) RD128(B[0][n],xc[0],n*2048);
  #pragma unroll
  for(int m=0;m<4;++m){ RDTR(A[0][m][0],wc[m&1][0],(m>>1)*512); RDTR(A[0][m][1],wc[m&1][1],(m>>1)*512); }
  WAITL(0);
  #pragma unroll 1
  for(int s=0;s<16;++s){
    {
      int r=0;
      #pragma unroll
      for(int m=0;m<4;++m)
      #pragma unroll
      for(int n=0;n<4;++n){
        M16(acc[m][n],cat(A[0][m][0],A[0][m][1]),B[0][n]);
        if(r<4){ RD128(B[1][r],xc[1],r*2048); }
        else if(r<12){ int mm=(r-4)>>1,t=(r-4)&1; RDTR(A[1][mm][t],wc[mm&1][t],8192+(mm>>1)*512); }
        ++r;
      }
    }
    if(PUB){ if(s==0){ WAITV(0); if(doflag) __hip_atomic_store(flag,tag,RLX_AGENT); } }
    WAITL(0);
    BAR();
    {
      unsigned d = ((s&3)==3) ? (unsigned)(-3*SLOT) : (unsigned)SLOT;
      xc[0]+=d; xc[1]+=d; wc[0][0]+=d; wc[0][1]+=d; wc[1][0]+=d; wc[1][1]+=d;
    }
    {
      int r=0;
      #pragma unroll
      for(int m=0;m<4;++m)
      #pragma unroll
      for(int n=0;n<4;++n){
        M16(acc[m][n],cat(A[1][m][0],A[1][m][1]),B[1][n]);
        if(r<4){ RD128(B[0][r],xc[0],r*2048); }
        else if(r<12){ int mm=(r-4)>>1,t=(r-4)&1; RDTR(A[0][mm][t],wc[mm&1][t],(mm>>1)*512); }
        ++r;
      }
    }
    WAITL(0);
  }
}

__device__ __forceinline__ bool poll_eq(gu64* p, u64 want, bool active){
  for(unsigned spins=0;spins<SPIN_CAP;++spins){
    u64 v = active ? __hip_atomic_load(p,RLX_AGENT) : want;
    if(__all(v==want)) return true;
    __builtin_amdgcn_s_sleep(1);
  }
  return false;
}

__global__ __launch_bounds__(512) void k_fused(Ptrs P){
  extern __shared__ __attribute__((aligned(16))) char smem[];
  const int lane=threadIdx.x&63, w=__builtin_amdgcn_readfirstlane(threadIdx.x>>6);
  const int b=blockIdx.x, g8=b&7, j=b>>3;
  const int br=g8>>2, oct=g8&3, jj=j&7, mb=j>>3;
  const int head=oct*8+jj;
  GAS char* ctl=(GAS char*)(P.ws+CTL_OFF);
  const int rgrp=(br*4+mb)*8+jj;
  const int pid = (lane<8) ? (g8+8*(mb*8+lane)) : ((br*4+(lane&3))+8*j);
  if(w>=4){
    const int lw=w-4;
    LAS char* L=(LAS char*)smem;
    const unsigned lb=(unsigned)(size_t)L;
    LST(0);
    const int rot=(4*mb+2*jj);
    const __amdgpu_buffer_rsrc_t rx=__builtin_amdgcn_make_buffer_rsrc((void*)((br?P.x1:P.x0)+(size_t)mb*128*1024),0,128*2048,0x00020000);
    const __amdgpu_buffer_rsrc_t rw=__builtin_amdgcn_make_buffer_rsrc((void*)((br?P.wq1:P.wq0)+head*128),0,1024*8192-head*256,0x00020000);
    const int vx=(lane>>3)*2048 + 16*((lane&7) ^ (((lane>>4)&3)|((lw&1)<<2)));
    const int X=((((lane>>4)&1)^(lw&1))<<1);
    const int vw=((lane>>2)&7)*8192 + (lane>>5)*64 + 16*((lane&3)^X);
    auto issue1=[&](int s){
      int st=(s+rot)&15; int slot=(s&3)*SLOT; int k0=st*64;
      #pragma unroll
      for(int u=0;u<4;++u){ int i=lw+4*u; __builtin_amdgcn_raw_ptr_buffer_load_lds(rx,(LAS void*)(L+slot+i*1024),16,vx,i*8*2048+k0*2,0,0); }
      #pragma unroll
      for(int u=0;u<4;++u){ int rg=lw+4*(u>>1), chh=u&1; int i=rg*2+chh; __builtin_amdgcn_raw_ptr_buffer_load_lds(rw,(LAS void*)(L+slot+16384+i*1024),16,vw,(k0+rg*8)*8192+chh*128,0,0); }
    };
    issue1(0); issue1(1);
    WAITV(8); BAR(); LST(1);
    issue1(2); issue1(3);
    const __amdgpu_buffer_rsrc_t rk=__builtin_amdgcn_make_buffer_rsrc((void*)((br?P.k1:P.k0)+(size_t)(mb*4*32+head)*4096),0,3*32*8192+8192,0x00020000);
    const __amdgpu_buffer_rsrc_t rv=__builtin_amdgcn_make_buffer_rsrc((void*)((br?P.v1:P.v0)+(size_t)(mb*4*32+head)*4096),0,3*32*8192+8192,0x00020000);
    const int vk=(lane>>4)*256 + 16*((lane&15) ^ (((lane>>4)<<2)|lw));
    v4u Kr[8], Vr[8];
    #pragma unroll 1
    for(int s=1;s<6;++s){ WAITV(16); BAR(); LST(1+s); issue1(s+3); }
    #pragma unroll
    for(int u=0;u<8;++u){ int so=(u>>1)*32*8192+(lw+4*(u&1))*1024; asm volatile("buffer_load_dwordx4 %0, %1, %2, %3 offen nt":"=v"(Kr[u]):"v"(vk),"s"(rk),"s"(so):"memory"); }
    #pragma unroll 1
    for(int s=6;s<9;++s){ WAITV(16); BAR(); LST(1+s); issue1(s+3); }
    #pragma unroll
    for(int u=0;u<8;++u){ int so=(u>>1)*32*8192+(lw+4*(u&1))*1024; asm volatile("buffer_load_dwordx4 %0, %1, %2, %3 offen nt":"=v"(Vr[u]):"v"(vk),"s"(rv),"s"(so):"memory"); }
    #pragma unroll 1
    for(int s=9;s<13;++s){ WAITV(16); BAR(); LST(1+s); issue1(s+3); }
    const __amdgpu_buffer_rsrc_t rwo=__builtin_amdgcn_make_buffer_rsrc((void*)((br?P.wo1:P.wo0)+(size_t)oct*1024*1024+jj*128),0,1024*2048-jj*256,0x00020000);
    const __amdgpu_buffer_rsrc_t ro=__builtin_amdgcn_make_buffer_rsrc((void*)(P.ws+O_OFF+((size_t)(br*4+mb)*64+oct*16)*16384),0,16*16384,0x00020000);
    const int vw2=((lane>>2)&7)*2048 + (lane>>5)*64 + 16*((lane&3)^X);
    auto st2=[&](int r)->int{
      if(r<2) return 2*jj+r;
      int t=((r-2)>>1)+2*mb; if(t>=7) t-=7; if(t>=7) t-=7;
      return 2*((jj+1+t)&7)+(r&1);
    };
    auto issue_wo=[&](int r){
      int slot=(r&3)*SLOT; int k0=st2(r)*64;
      #pragma unroll
      for(int u=0;u<4;++u){ int rg=lw+4*(u>>1), chh=u&1; int i=rg*2+chh; __builtin_amdgcn_raw_ptr_buffer_load_lds(rwo,(LAS void*)(L+slot+16384+i*1024),16,vw2,(k0+rg*8)*2048+chh*128,0,0); }
    };
    auto issue_o=[&](int r){
      int slot=(r&3)*SLOT; int so=st2(r)*16384;
      #pragma unroll
      for(int u=0;u<4;++u){ int i=lw+4*u; __builtin_amdgcn_raw_ptr_buffer_load_lds(ro,(LAS void*)(L+slot+i*1024),16,lane*16,so+i*1024,0,SC1); }
    };
    const unsigned kva=lb+lw*1024+lane*16;
    WAITV(16); BAR(); LST(14);
    #pragma unroll
    for(int u=0;u<8;++u){ asm volatile("":"+v"(Kr[u])); asm volatile("":"+v"(Vr[u])); }
    #pragma unroll
    for(int u=0;u<4;++u) WR128(kva,(u>>1)*8192+(u&1)*4096,Kr[u]);
    issue_wo(0);
    WAITV(12); BAR(); LST(15);
    #pragma unroll
    for(int u=4;u<8;++u) WR128(kva,SLOT+((u-4)>>1)*8192+(u&1)*4096,Kr[u]);
    issue_wo(1);
    WAITV(8); WAITL(0); BAR(); LST(16);
    #pragma unroll
    for(int u=0;u<4;++u) asm volatile("ds_write_b128 %0, %1 offset:%2"::"v"(kva+2*SLOT),"v"(Vr[u]),"i"((u>>1)*8192+(u&1)*4096):"memory");
    issue_wo(2);
    WAITL(0); BAR(); LST(17);
    #pragma unroll
    for(int u=4;u<8;++u) asm volatile("ds_write_b128 %0, %1 offset:%2"::"v"(kva+3*SLOT),"v"(Vr[u]),"i"(((u-4)>>1)*8192+(u&1)*4096):"memory");
    issue_wo(3);
    WAITL(0); BAR(); LST(18);
    WAITV(12); BAR(); LST(19);
    WAITV(8); BAR(); LST(20);
    const u64 myn=*(volatile LAS u64*)(L+NLDS);
    poll_eq((gu64*)(ctl+OIN_OFF+b*256+(lane&31)*8),myn,(lane<32)&&((lane>>2)!=jj));
    LST(21);
    issue_o(2); issue_o(3); issue_o(4); issue_wo(4);
    u64 fresh;
    const int pj=(lane>>2)&7;
    { gu64* pp=(gu64*)(ctl+R_OFF+(g8+8*(mb*8+pj))*128); asm volatile("global_load_dwordx2 %0, %1, off sc1":"=v"(fresh):"v"(pp):"memory"); }
    WAITV(13); BAR(); LST(22);
    issue_o(5); issue_wo(5);
    WAITV(17); BAR(); LST(23);
    issue_o(6); issue_wo(6);
    asm volatile("s_waitcnt vmcnt(16)":"+v"(fresh)::"memory");
    BAR();
    if(lw==0 && lane<32 && pj!=jj) __hip_atomic_store((gu64*)(ctl+OIN_OFF+(g8+8*(mb*8+pj))*256+(jj*4+(lane&3))*8),fresh,RLX_AGENT);
    #pragma unroll 1
    for(int r=5;r<14;++r){ issue_o(r+2); issue_wo(r+2); WAITV(16); BAR(); }
    LST(24);
    WAITV(8); BAR();
    WAITV(0); BAR();
    LST(25);
    BAR();
    BAR();
    if(w==4&&lane<48) ((GAS unsigned*)(P.ws+(64u<<20)+blockIdx.x*256))[lane]=((volatile LAS unsigned*)((LAS char*)smem+LDS_BYTES))[lane];
    return;
  }
  const unsigned lb=(unsigned)(size_t)(LAS char*)smem;
  const int g=lane>>4, i16=lane&15, q=i16>>2, p=lane&3;
  const int wm=w>>1, wn=w&1;
  unsigned xc[2], wc[2][2];
  { int row=wm*64+i16; int s=(row>>1)&7; xc[0]=lb+128*row+16*((g)^s); xc[1]=lb+128*row+16*((4+g)^s); }
  #pragma unroll
  for(int c1=0;c1<2;++c1)
  #pragma unroll
  for(int t=0;t<2;++t){ int X=((t^g)&1)<<1; wc[c1][t]=lb+16384+2048*g+64*(4*t+q)+16*((2*c1+(p>>1))^X)+8*(p&1)+1024*wn; }
  f32x4 acc[4][4];
  #pragma unroll
  for(int m=0;m<4;++m)
  #pragma unroll
  for(int n=0;n<4;++n) acc[m][n]=(f32x4){0,0,0,0};
  if(w==0){
    gu64* rp_=(gu64*)(ctl+R_OFF+b*128);
    u64 old=__hip_atomic_load(rp_,RLX_AGENT);
    u64 n=old*0x9E3779B97F4A7C15ull+0x632BE59BD9B4E019ull;
    if(lane==0){ __hip_atomic_store(rp_,n,RLX_AGENT); *(volatile LAS u64*)((LAS char*)smem+NLDS)=n; }
    WAITL(0);
  }
  BAR();
  CST(30);
  gemm_loop<false>(acc,xc,wc,nullptr,0,false);
  u64 pn;
  { gu64* pp=(gu64*)(ctl+R_OFF+pid*128); asm volatile("global_load_dwordx2 %0, %1, off sc1":"=v"(pn):"v"(pp):"memory"); }
  CST(31);
  v2u Kf[2][2][2][2];
  {
    const int f=((i16&3)<<2)|((i16>>2)&3);
    #pragma unroll
    for(int mm=0;mm<2;++mm)
    #pragma unroll
    for(int t=0;t<2;++t){
      unsigned a=lb+wm*SLOT+256*i16+16*(((2*wn+mm)*4+2*t+(g>>1))^f)+8*(g&1);
      #pragma unroll
      for(int l=0;l<2;++l)
      #pragma unroll
      for(int kb=0;kb<2;++kb) RD64(Kf[l][kb][mm][t],a,l*8192+kb*4096);
    }
  }
  #pragma unroll
  for(int m=0;m<4;++m) FENCE4("s_nop 1",acc[m][0],acc[m][1],acc[m][2],acc[m][3]);
  v4u QB[4][2];
  #pragma unroll
  for(int n=0;n<4;++n)
  #pragma unroll
  for(int mm=0;mm<2;++mm){
    QB[n][mm]=(v4u){pk(acc[2*mm][n][0],acc[2*mm][n][1]),pk(acc[2*mm][n][2],acc[2*mm][n][3]),pk(acc[2*mm+1][n][0],acc[2*mm+1][n][1]),pk(acc[2*mm+1][n][2],acc[2*mm+1][n][3])};
  }
  WAITL(0);
  FENCE4("s_nop 1",QB[0][0],QB[0][1],QB[1][0],QB[1][1]);
  FENCE4("s_nop 1",QB[2][0],QB[2][1],QB[3][0],QB[3][1]);
  f32x4 S[2][2][2];
  #pragma unroll
  for(int l=0;l<2;++l)
  #pragma unroll
  for(int kb=0;kb<2;++kb)
  #pragma unroll
  for(int qb=0;qb<2;++qb) M16Z(S[l][kb][qb],cat(Kf[l][kb][0][0],Kf[l][kb][0][1]),QB[2*l+qb][0]);
  #pragma unroll
  for(int l=0;l<2;++l)
  #pragma unroll
  for(int kb=0;kb<2;++kb)
  #pragma unroll
  for(int qb=0;qb<2;++qb) M16(S[l][kb][qb],cat(Kf[l][kb][1][0],Kf[l][kb][1][1]),QB[2*l+qb][1]);
  FENCE4("s_nop 7",S[0][0][0],S[0][0][1],S[0][1][0],S[0][1][1]);
  FENCE4("s_nop 0",S[1][0][0],S[1][0][1],S[1][1][0],S[1][1][1]);
  {
    LAS char* sc=(LAS char*)smem+SCR+w*4096+lane*16;
    #pragma unroll
    for(int kb=0;kb<2;++kb)
    #pragma unroll
    for(int qb=0;qb<2;++qb) *(LAS f32x4*)(sc+(kb*2+qb)*1024) = wn? S[0][kb][qb] : S[1][kb][qb];
  }
  WAITL(0);
  asm volatile("":::"memory");
  BAR();
  CST(32);
  f32x4 T[2][2];
  {
    LAS char* sc=(LAS char*)smem+SCR+(w^1)*4096+lane*16;
    #pragma unroll
    for(int kb=0;kb<2;++kb)
    #pragma unroll
    for(int qb=0;qb<2;++qb){ f32x4 o=*(LAS f32x4*)(sc+(kb*2+qb)*1024); T[kb][qb]=(wn? S[1][kb][qb] : S[0][kb][qb]) + o; }
  }
  v2u Vf[8][2];
  {
    const int f=(q<<2)|g;
    #pragma unroll
    for(int u=0;u<4;++u){
      unsigned a=lb+(2+wm)*SLOT+wn*8192+256*(4*g+q)+16*((4*u+p)^f);
      #pragma unroll
      for(int hf=0;hf<2;++hf)
      #pragma unroll
      for(int t=0;t<2;++t) RDTR(Vf[2*u+hf][t],a,t*4096+hf*8);
    }
  }
  v4u PB[2];
  constexpr float SCL=0.08838834764831845f*1.4426950408889634f;
  #pragma unroll
  for(int qb=0;qb<2;++qb){
    float mx=fmaxf(fmaxf(fmaxf(T[0][qb][0],T[0][qb][1]),fmaxf(T[0][qb][2],T[0][qb][3])),fmaxf(fmaxf(T[1][qb][0],T[1][qb][1]),fmaxf(T[1][qb][2],T[1][qb][3])));
    { auto r=__builtin_amdgcn_permlane16_swap(__float_as_uint(mx),__float_as_uint(mx),false,false); mx=fmaxf(__uint_as_float(r[0]),__uint_as_float(r[1])); }
    { auto r=__builtin_amdgcn_permlane32_swap(__float_as_uint(mx),__float_as_uint(mx),false,false); mx=fmaxf(__uint_as_float(r[0]),__uint_as_float(r[1])); }
    float e[8]; float sum=0; const float nm=-mx*SCL;
    #pragma unroll
    for(int kb=0;kb<2;++kb)
    #pragma unroll
    for(int c=0;c<4;++c){ e[kb*4+c]=__builtin_amdgcn_exp2f(__builtin_fmaf(T[kb][qb][c],SCL,nm)); sum+=e[kb*4+c]; }
    { auto r=__builtin_amdgcn_permlane16_swap(__float_as_uint(sum),__float_as_uint(sum),false,false); sum=__uint_as_float(r[0])+__uint_as_float(r[1]); }
    { auto r=__builtin_amdgcn_permlane32_swap(__float_as_uint(sum),__float_as_uint(sum),false,false); sum=__uint_as_float(r[0])+__uint_as_float(r[1]); }
    float inv=__builtin_amdgcn_rcpf(sum);
    PB[qb]=(v4u){pk(e[0]*inv,e[1]*inv),pk(e[2]*inv,e[3]*inv),pk(e[4]*inv,e[5]*inv),pk(e[6]*inv,e[7]*inv)};
  }
  WAITL(0);
  asm volatile("s_nop 1":"+v"(PB[0]),"+v"(PB[1]));
  f32x4 O[8][2];
  #pragma unroll
  for(int m=0;m<8;++m)
  #pragma unroll
  for(int qb=0;qb<2;++qb) M16Z(O[m][qb],cat(Vf[m][0],Vf[m][1]),PB[qb]);
  FENCE4("s_nop 7",O[6][0],O[6][1],O[7][0],O[7][1]);
  #pragma unroll
  for(int m=0;m<6;m+=2) FENCE4("",O[m][0],O[m][1],O[m+1][0],O[m+1][1]);
  {
    const __amdgpu_buffer_rsrc_t rs=__builtin_amdgcn_make_buffer_rsrc((void*)(P.ws+O_OFF+((size_t)(br*4+mb)*64+2*head)*16384),0,2*16384,0x00020000);
    const int bb=2*wm+wn;
    const unsigned oa=lb+bb*4096+lane*16;
    #pragma unroll
    for(int mm=0;mm<4;++mm)
    #pragma unroll
    for(int qb=0;qb<2;++qb){
      v4u o=(v4u){pk(O[2*mm][qb][0],O[2*mm][qb][1]),pk(O[2*mm][qb][2],O[2*mm][qb][3]),pk(O[2*mm+1][qb][0],O[2*mm+1][qb][1]),pk(O[2*mm+1][qb][2],O[2*mm+1][qb][3])};
      __builtin_amdgcn_raw_buffer_store_b128(o,rs,bb*4096+lane*16,(mm>>1)*16384+qb*2048+(mm&1)*1024,SC1);
      WR128(oa,(mm>>1)*SLOT+qb*2048+(mm&1)*1024,o);
    }
  }
  xc[0]=lb+lane*16+wm*8192; xc[1]=xc[0]+1024;
  #pragma unroll
  for(int c1=0;c1<2;++c1)
  #pragma unroll
  for(int t=0;t<2;++t){ int X=((t^g)&1)<<1; wc[c1][t]=lb+16384+2048*g+64*(4*t+q)+16*((2*c1+(p>>1))^X)+8*(p&1)+1024*wn; }
  #pragma unroll
  for(int m=0;m<4;++m)
  #pragma unroll
  for(int n=0;n<4;++n) acc[m][n]=(f32x4){0,0,0,0};
  WAITL(0);
  asm volatile("s_waitcnt vmcnt(8)":"+v"(pn)::"memory");
  CST(33);
  BAR();
  CST(34);
  gemm_loop<true>(acc,xc,wc,(gu64*)(ctl+OIN_OFF+pid*256+(jj*4+w)*8),pn,(lane<8)&&(lane!=jj));
  CST(35);
  #pragma unroll
  for(int m=0;m<4;++m) FENCE4("s_nop 1",acc[m][0],acc[m][1],acc[m][2],acc[m][3]);
  {
    constexpr int TB = PART_BF16? 512:1024;
    const __amdgpu_buffer_rsrc_t rp=__builtin_amdgcn_make_buffer_rsrc((void*)(P.ws+PART_OFF+(size_t)rgrp*(4*4*4*4*TB)),0,4*4*4*4*TB,0x00020000);
    #pragma unroll
    for(int n=0;n<4;++n){
      if(n!=oct){
        #pragma unroll
        for(int m=0;m<4;m+=(PART_BF16?2:1)){
          if(PART_BF16){
            v4u o=(v4u){pk(acc[m][n][0],acc[m][n][1]),pk(acc[m][n][2],acc[m][n][3]),pk(acc[m+1][n][0],acc[m+1][n][1]),pk(acc[m+1][n][2],acc[m+1][n][3])};
            __builtin_amdgcn_raw_buffer_store_b128(o,rp,lane*16,(((n*4+oct)*4+w)*4+m)*TB,SC1);
          } else {
            __builtin_amdgcn_raw_buffer_store_b128(__builtin_bit_cast(v4u,acc[m][n]),rp,lane*16,(((n*4+oct)*4+w)*4+m)*TB,SC1);
          }
        }
      }
    }
    WAITV(0);
    const bool rpart=(lane>=8)&&(lane<12)&&((lane&3)!=oct);
    gu64* rflag=(gu64*)(ctl+RIN_OFF+pid*128+(oct*4+w)*8);
    if(rpart) __hip_atomic_store(rflag,pn,RLX_AGENT);
    CST(36);
    f32x4 mine[4];
    #pragma unroll
    for(int m=0;m<4;++m) mine[m]= oct==0?acc[m][0]: oct==1?acc[m][1]: oct==2?acc[m][2]:acc[m][3];
    const u64 myn=*(volatile LAS u64*)((LAS char*)smem+NLDS);
    poll_eq((gu64*)(ctl+RIN_OFF+b*128+((lane&3)*4+w)*8),myn,(lane<4)&&(lane!=oct));
    CST(37);
    const u64 fresh=__hip_atomic_load((gu64*)(ctl+R_OFF+pid*128),RLX_AGENT);
    v4u in[3][PART_BF16?2:4];
    #pragma unroll
    for(int e=0;e<3;++e){
      int src=e+(e>=oct?1:0);
      #pragma unroll
      for(int m=0;m<(PART_BF16?2:4);++m) in[e][m]=__builtin_amdgcn_raw_buffer_load_b128(rp,lane*16,(((oct*4+src)*4+w)*4+(PART_BF16?2*m:m))*TB,SC1);
    }
    #pragma unroll
    for(int e=0;e<3;++e){
      if(PART_BF16){
        #pragma unroll
        for(int m=0;m<2;++m){
          mine[2*m][0]+=bflo(in[e][m][0]); mine[2*m][1]+=bfhi(in[e][m][0]); mine[2*m][2]+=bflo(in[e][m][1]); mine[2*m][3]+=bfhi(in[e][m][1]);
          mine[2*m+1][0]+=bflo(in[e][m][2]); mine[2*m+1][1]+=bfhi(in[e][m][2]); mine[2*m+1][2]+=bflo(in[e][m][3]); mine[2*m+1][3]+=bfhi(in[e][m][3]);
        }
      } else {
        #pragma unroll
        for(int m=0;m<4;++m) mine[m]+=__builtin_bit_cast(f32x4,in[e][m]);
      }
    }
    if(rpart) __hip_atomic_store(rflag,fresh,RLX_AGENT);
    u16* op=P.out+(size_t)(mb*128+wm*64+16*oct+i16)*2048+br*1024+jj*128+wn*64+4*g;
    #pragma unroll
    for(int m=0;m<4;++m) *(GAS v2u*)(op+16*m)=(v2u){pk(mine[m][0],mine[m][1]),pk(mine[m][2],mine[m][3])};
    CST(38);
  }
  BAR();
}

__global__ void k_spin(Ptrs P,int idx,int mult){
  const GAS unsigned* h=(const GAS unsigned*)(P.ws+(64u<<20));
  unsigned t0v=h[threadIdx.x*64+0], tv=h[threadIdx.x*64+idx];
  __shared__ unsigned a[256],c[256];
  a[threadIdx.x]=t0v; c[threadIdx.x]=tv; __syncthreads();
  if(threadIdx.x==0){
    unsigned mn=a[0]; for(int i=1;i<256;++i) if((int)(a[i]-mn)<0) mn=a[i];
    unsigned mx=0; for(int i=0;i<256;++i){ unsigned d=c[i]-mn; if(d>mx) mx=d; }
    unsigned lim=mx*mult; if(lim>100000u) lim=100000u;
    unsigned long long r0,r; asm volatile("s_memrealtime %0\n\ts_waitcnt lgkmcnt(0)":"=s"(r0)::"memory");
    do{ asm volatile("s_memrealtime %0\n\ts_waitcnt lgkmcnt(0)":"=s"(r)::"memory"); }while((unsigned)(r-r0)<lim);
  }
}
extern "C" void kernel_launch(void* const* d_in, const int* in_sizes, int n_in,
                              void* d_out, int out_size, void* d_ws, size_t ws_size,
                              hipStream_t stream) {
  Ptrs P;
  P.x0=(const u16*)d_in[0]; P.k0=(const u16*)d_in[1]; P.v0=(const u16*)d_in[2]; P.wq0=(const u16*)d_in[3]; P.wo0=(const u16*)d_in[4];
  P.x1=(const u16*)d_in[5]; P.k1=(const u16*)d_in[6]; P.v1=(const u16*)d_in[7]; P.wq1=(const u16*)d_in[8]; P.wo1=(const u16*)d_in[9];
  P.out=(u16*)d_out; P.ws=(char*)d_ws;
  const int lds=LDS_BYTES+256;
  (void)hipFuncSetAttribute((const void*)k_fused,hipFuncAttributeMaxDynamicSharedMemorySize,lds);
  k_fused<<<256,512,lds,stream>>>(P);
  k_spin<<<1,256,0,stream>>>(P,0,0);
}
```

```cpp
#define KVMODE 0
#define EARLYPUB 1
#define CODEPF 1
#include <hip/hip_runtime.h>
#include <hip/hip_bf16.h>
typedef unsigned v4u __attribute__((ext_vector_type(4)));
typedef unsigned v2u __attribute__((ext_vector_type(2)));
typedef float f32x4 __attribute__((ext_vector_type(4)));
typedef unsigned short u16;
typedef unsigned long long u64;
#define GAS __attribute__((address_space(1)))
#define LAS __attribute__((address_space(3)))
typedef GAS u64 gu64;
#define M16(c,a,b) asm volatile("v_mfma_f32_16x16x32_bf16 %0, %1, %2, %0":"+v"(c):"v"(a),"v"(b))
#define M16Z(c,a,b) asm volatile("v_mfma_f32_16x16x32_bf16 %0, %1, %2, 0":"=v"(c):"v"(a),"v"(b))
#define FENCE4(n,a,b,c,d) asm volatile(n:"+v"(a),"+v"(b),"+v"(c),"+v"(d))
#define RD128(d,addr,off) asm volatile("ds_read_b128 %0, %1 offset:%2":"=v"(d):"v"(addr),"i"(off))
#define RD64(d,addr,off) asm volatile("ds_read_b64 %0, %1 offset:%2":"=v"(d):"v"(addr),"i"(off))
#define RDTR(d,addr,off) asm volatile("ds_read_b64_tr_b16 %0, %1 offset:%2":"=v"(d):"v"(addr),"i"(off))
#define WR128(addr,off,d) asm volatile("ds_write_b128 %0, %1 offset:%2"::"v"(addr),"v"(d),"i"(off):"memory")
#define WAITL(n) asm volatile("s_waitcnt lgkmcnt(%0)"::"i"(n):"memory")
#define WAITV(n) asm volatile("s_waitcnt vmcnt(%0)"::"i"(n):"memory")
#define BAR() asm volatile("s_barrier":::"memory")
#define RLX_AGENT __ATOMIC_RELAXED, __HIP_MEMORY_SCOPE_AGENT
constexpr int SC1=16;
constexpr int SLOT=32768;
constexpr int SCR=4*SLOT;
constexpr int NLDS=SCR+16384;
constexpr int LDS_BYTES=NLDS+64;
constexpr size_t O_OFF=0;
constexpr size_t PART_OFF=(size_t)8<<20;
constexpr size_t CTL_OFF=(size_t)32<<20;
constexpr int R_OFF=0;
constexpr int OIN_OFF=256*128;
constexpr int RIN_OFF=OIN_OFF+256*256;
constexpr unsigned SPIN_CAP=1u<<16;

#define CST(i)
#define LCY(i)
#define LST(i)

__device__ __forceinline__ v4u cat(v2u a,v2u b){ return __builtin_shufflevector(a,b,0,1,2,3); }
__device__ __forceinline__ unsigned pk(float a,float b){ unsigned r; asm("v_cvt_pk_bf16_f32 %0, %1, %2":"=v"(r):"v"(a),"v"(b)); return r; }
__device__ __forceinline__ float bflo(unsigned u){ return __uint_as_float(u<<16); }
__device__ __forceinline__ float bfhi(unsigned u){ return __uint_as_float(u&0xffff0000u); }

#define DMA(rs,dst,voff,soff,u,aux) do{ if((u)==0) __builtin_amdgcn_raw_ptr_buffer_load_lds(rs,(LAS void*)(dst),16,voff,soff,0,aux); \
  else if((u)==1) __builtin_amdgcn_raw_ptr_buffer_load_lds(rs,(LAS void*)(dst),16,voff,soff,1024,aux); \
  else if((u)==2) __builtin_amdgcn_raw_ptr_buffer_load_lds(rs,(LAS void*)(dst),16,voff,soff,2048,aux); \
  else __builtin_amdgcn_raw_ptr_buffer_load_lds(rs,(LAS void*)(dst),16,voff,soff,3072,aux); }while(0)
struct Ptrs { const u16 *x0,*x1,*k0,*k1,*v0,*v1,*wq0,*wq1,*wo0,*wo1; u16* out; char* ws; };

template<int PH> __device__ __forceinline__ void rdB(v4u& d, unsigned (&xa)[4], int n, int kk){
  if(PH==0){ if(n&1) RD128(d,xa[kk*2+(n>>1)],2048); else RD128(d,xa[kk*2+(n>>1)],0); }
  else { if(kk) RD128(d,xa[n],1024); else RD128(d,xa[n],0); }
}
template<int KK> __device__ __forceinline__ void rdA(v2u& d, unsigned (&wc)[2][2], int m, int t){
  if(m>>1) RDTR(d,wc[m&1][t],KK*8192+512); else RDTR(d,wc[m&1][t],KK*8192);
}

template<int PH>
__device__ __forceinline__ void gemm_loop(f32x4 (&acc)[4][4], unsigned (&xa)[4], unsigned (&wc)[2][2], gu64* flag, u64 tag, bool doflag){
  v2u A[2][4][2]; v4u B[2][4];
  #pragma unroll
  for(int n=0;n<4;++n) rdB<PH>(B[0][n],xa,n,0);
  #pragma unroll
  for(int m=0;m<4;++m){ rdA<0>(A[0][m][0],wc,m,0); rdA<0>(A[0][m][1],wc,m,1); }
  #pragma unroll 1
  for(int s=0;s<16;++s){
    #pragma unroll
    for(int m=0;m<4;++m){
      if(m==0) WAITL(6); else if(m==1) WAITL(8); else if(m==2) WAITL(10); else WAITL(12);
      #pragma unroll
      for(int n=0;n<4;++n){
        M16(acc[m][n],cat(A[0][m][0],A[0][m][1]),B[0][n]);
        if(m==0) rdB<PH>(B[1][n],xa,n,1);
        else if(m<3){ int r=(m-1)*4+n; rdA<1>(A[1][r>>1][r&1],wc,r>>1,r&1); }
      }
    }
    WAITL(6);
    #pragma unroll
    for(int n=0;n<4;++n) M16(acc[0][n],cat(A[1][0][0],A[1][0][1]),B[1][n]);
    WAITL(0);
    BAR();
    {
      unsigned d = ((s&3)==3) ? (unsigned)(-3*SLOT) : (unsigned)SLOT;
      xa[0]+=d; xa[1]+=d; xa[2]+=d; xa[3]+=d; wc[0][0]+=d; wc[0][1]+=d; wc[1][0]+=d; wc[1][1]+=d;
    }
    #pragma unroll
    for(int m=1;m<4;++m){
      #pragma unroll
      for(int n=0;n<4;++n){
        M16(acc[m][n],cat(A[1][m][0],A[1][m][1]),B[1][n]);
        if(m==1) rdB<PH>(B[0][n],xa,n,0);
        else { int r=(m-2)*4+n; rdA<0>(A[0][r>>1][r&1],wc,r>>1,r&1); }
      }
    }
  }
  WAITL(0);
}

__device__ __forceinline__ bool poll_eq(gu64* p, u64 want, bool active){
  for(unsigned spins=0;spins<SPIN_CAP;++spins){
    u64 v = active ? __hip_atomic_load(p,RLX_AGENT) : want;
    if(__all(v==want)) return true;
    __builtin_amdgcn_s_sleep(1);
  }
  return false;
}

__global__ __launch_bounds__(512) void k_fused(Ptrs P){
  extern __shared__ __attribute__((aligned(16))) char smem[];
  const int lane=threadIdx.x&63, w=__builtin_amdgcn_readfirstlane(threadIdx.x>>6);
  const int b=blockIdx.x, g8=b&7, j=b>>3;
  const int br=g8>>2, oct=g8&3, jj=j&7, mb=j>>3;
  const int head=oct*8+jj;
  GAS char* ctl=(GAS char*)(P.ws+CTL_OFF);
  if(w>=4){
    const int lw=w-4;
    LAS char* L=(LAS char*)smem;
    const unsigned lb=(unsigned)(size_t)L;
    LST(0);
    const int rot=(4*mb+2*jj);
    const __amdgpu_buffer_rsrc_t rx=__builtin_amdgcn_make_buffer_rsrc((void*)((br?P.x1:P.x0)+(size_t)mb*128*1024),0,128*2048,0x00020000);
    const __amdgpu_buffer_rsrc_t rw=__builtin_amdgcn_make_buffer_rsrc((void*)((br?P.wq1:P.wq0)+head*128-2048),0,1024*8192-head*256+4096,0x00020000);
    int vx[4], vw[4];
    #pragma unroll
    for(int u=0;u<4;++u){
      vx[u]=(lane>>3)*2048 + 16*((lane&7) ^ (((lane>>4)&3)|((u&1)<<2))) + u*(8*2048-1024);
      const int X=((((lane>>4)&1)^(u>>1))<<1);
      vw[u]=((lane>>2)&7)*8192 + (lane>>5)*64 + 16*((lane&3)^X) + (u>>1)*(8*8192) + (u&1)*128 - u*1024 + 4096;
    }
    auto issue1=[&](int s){
      const int st=(s+rot)&15; LAS char* d=L+(s&3)*SLOT+lw*4096;
      const int sx=lw*(4*8*2048)+st*128, sw=(st*64+lw*16)*8192;
      #pragma unroll
      for(int u=0;u<4;++u) DMA(rx,d,vx[u],sx,u,0);
      #pragma unroll
      for(int u=0;u<4;++u) DMA(rw,d+16384,vw[u],sw,u,0);
    };
    issue1(0); issue1(1);
    WAITV(8); BAR(); LST(1); LCY(43);
    issue1(2); issue1(3);
    const __amdgpu_buffer_rsrc_t rk=__builtin_amdgcn_make_buffer_rsrc((void*)((br?P.k1:P.k0)+(size_t)(mb*4*32+head)*4096),0,3*32*8192+8192,0x00020000);
    const __amdgpu_buffer_rsrc_t rv=__builtin_amdgcn_make_buffer_rsrc((void*)((br?P.v1:P.v0)+(size_t)(mb*4*32+head)*4096),0,3*32*8192+8192,0x00020000);
    const int vk=(lane>>4)*256 + 16*((lane&15) ^ (((lane>>4)<<2)|lw));
    v4u Kr[8], Vr[8];
    #pragma unroll
    for(int u=0;u<8;++u){ Kr[u]=(v4u){0,0,0,0}; Vr[u]=(v4u){0,0,0,0}; }
    const int rK=4+(j&3), rV=rK+4;
    int c_st=(4+rot)&15; unsigned c_d=lb+lw*4096;
    int c_sx=lw*(4*8*2048)+c_st*128, c_sw=(c_st*64+lw*16)*8192;
    #pragma unroll 1
    for(int s=1;s<13;++s){
      WAITV(16); BAR();
      #pragma unroll
      for(int u=0;u<4;++u) DMA(rx,(LAS char*)(size_t)c_d,vx[u],c_sx,u,0);
      __builtin_amdgcn_sched_barrier(0);
      const unsigned w_d=c_d+16384; const int w_s=c_sw;
      c_st=(c_st+1)&15; c_d=lb+lw*4096+((s+4)&3)*SLOT;
      c_sx=lw*(4*8*2048)+c_st*128; c_sw=(c_st*64+lw*16)*8192;
      asm volatile("":"+s"(c_d),"+s"(c_sx),"+s"(c_sw));
      __builtin_amdgcn_sched_barrier(0);
      #pragma unroll
      for(int u=0;u<4;++u) DMA(rw,(LAS char*)(size_t)w_d,vw[u],w_s,u,0);
      __builtin_amdgcn_sched_barrier(0);
      if(s==rK){
        #pragma unroll
        for(int u=0;u<8;++u){ int so=(u>>1)*32*8192+(lw+4*(u&1))*1024; asm volatile("buffer_load_dwordx4 %0, %1, %2, %3 offen nt":"+v"(Kr[u]):"v"(vk),"s"(rk),"s"(so):"memory"); }
      }
      if(s==rV){
        #pragma unroll
        for(int u=0;u<8;++u){ int so=(u>>1)*32*8192+(lw+4*(u&1))*1024; asm volatile("buffer_load_dwordx4 %0, %1, %2, %3 offen nt":"+v"(Vr[u]):"v"(vk),"s"(rv),"s"(so):"memory"); }
      }
    }
    LST(13); LCY(44);
    const __amdgpu_buffer_rsrc_t rwo=__builtin_amdgcn_make_buffer_rsrc((void*)((br?P.wo1:P.wo0)+(size_t)oct*1024*1024+jj*128-2048),0,1024*2048-jj*256+4096,0x00020000);
    const __amdgpu_buffer_rsrc_t ro=__builtin_amdgcn_make_buffer_rsrc((void*)(P.ws+O_OFF+((size_t)(br*4+mb)*64+oct*16)*16384),0,16*16384,0x00020000);
    int vw2[4];
    #pragma unroll
    for(int u=0;u<4;++u){
      const int X=((((lane>>4)&1)^(u>>1))<<1);
      vw2[u]=((lane>>2)&7)*2048 + (lane>>5)*64 + 16*((lane&3)^X) + (u>>1)*(8*2048) + (u&1)*128 - u*1024 + 4096;
    }
    auto hd2=[&](int r)->int{
      int t=((r-2)>>1)+2*mb; if(t>=7) t-=7; if(t>=7) t-=7;
      return (jj+1+t)&7;
    };
    auto st2=[&](int r)->int{ return r<2 ? 2*jj+r : 2*hd2(r)+(r&1); };
    auto issue_wo=[&](int r){
      LAS char* d=L+(r&3)*SLOT+16384+lw*4096; const int so=(st2(r)*64+lw*16)*2048;
      #pragma unroll
      for(int u=0;u<4;++u) DMA(rwo,d,vw2[u],so,u,0);
    };
    auto issue_o=[&](int r){
      LAS char* d=L+(r&3)*SLOT+lw*4096; const int so=st2(r)*16384+lw*4096;
      #pragma unroll
      for(int u=0;u<4;++u) DMA(ro,d,lane*16,so,u,SC1);
    };
    const unsigned kva=lb+lw*1024+lane*16;
    WAITV(16); BAR(); LST(14);
    #pragma unroll
    for(int u=0;u<4;++u) WR128(kva,(u>>1)*8192+(u&1)*4096,Kr[u]);
    issue_wo(0);
    WAITV(12); BAR();
    #pragma unroll
    for(int u=4;u<8;++u) WR128(kva,SLOT+((u-4)>>1)*8192+(u&1)*4096,Kr[u]);
    issue_wo(1);
    WAITV(8); WAITL(0); BAR();
    #pragma unroll
    for(int u=0;u<4;++u) WR128(kva+2*SLOT,(u>>1)*8192+(u&1)*4096,Vr[u]);
    issue_wo(2);
    WAITL(0); BAR(); LST(17);
    #pragma unroll
    for(int u=4;u<8;++u) WR128(kva+3*SLOT,((u-4)>>1)*8192+(u&1)*4096,Vr[u]);
    WAITL(0); BAR(); LST(18);
    issue_wo(3);
    WAITV(12); BAR(); LST(19);
    WAITV(8); BAR(); LST(20);
    const u64 myn=*(volatile LAS u64*)(L+NLDS);
    gu64* inbox=(gu64*)(ctl+OIN_OFF+b*256+(lane&31)*8);
    unsigned ready=0xFu<<(4*jj);
    u64 pv=~myn;
    auto need_head=[&](int h){
      asm volatile("":"+v"(pv));
      ready|=(unsigned)__ballot(pv==myn);
      for(unsigned spins=0; ((ready>>(4*h))&15u)!=15u && spins<SPIN_CAP; ++spins){
        u64 v=__hip_atomic_load(inbox,RLX_AGENT);
        ready|=(unsigned)__ballot(v==myn);
        if(((ready>>(4*h))&15u)!=15u) __builtin_amdgcn_s_sleep(1);
      }
      if(ready!=0xFFFFFFFFu) asm volatile("global_load_dwordx2 %0, %1, off sc1":"+v"(pv):"v"(inbox):"memory");
    };
    need_head(hd2(2));
    LST(21);
    issue_o(2); issue_o(3); issue_wo(4);
    WAITV(8); BAR(); LST(22);
    need_head(hd2(4));
    issue_o(4); issue_o(5); issue_wo(5);
    WAITV(12); BAR(); LST(23); LCY(41);
    need_head(hd2(6));
    int n_st=st2(6); unsigned n_d=lb+lw*4096+(6&3)*SLOT;
    #pragma unroll 1
    for(int r=4;r<14;++r){
      const int so_o=n_st*16384+lw*4096, so_w=(n_st*64+lw*16)*2048; const unsigned d_o=n_d;
      #pragma unroll
      for(int u=0;u<4;++u) DMA(ro,(LAS char*)(size_t)d_o,lane*16,so_o,u,SC1);
      __builtin_amdgcn_sched_barrier(0);
      #pragma unroll
      for(int u=0;u<2;++u) DMA(rwo,(LAS char*)(size_t)(d_o+16384),vw2[u],so_w,u,0);
      __builtin_amdgcn_sched_barrier(0);
      if(r<13){
        n_st=st2(r+3); n_d=lb+lw*4096+((r+3)&3)*SLOT;
        if(r&1){ asm volatile("s_waitcnt vmcnt(14)":"+v"(pv)::"memory"); need_head(n_st>>1); }
      }
      asm volatile("":"+s"(n_st),"+s"(n_d));
      __builtin_amdgcn_sched_barrier(0);
      #pragma unroll
      for(int u=2;u<4;++u) DMA(rwo,(LAS char*)(size_t)(d_o+16384),vw2[u],so_w,u,0);
      __builtin_amdgcn_sched_barrier(0);
      WAITV(16); BAR();
    }
    LST(24); LCY(42);
    {
      const int pj=(lane>>2)&7;
      u64 fresh;
      { gu64* pp=(gu64*)(ctl+R_OFF+(g8+8*(mb*8+pj))*128); asm volatile("global_load_dwordx2 %0, %1, off sc1":"=v"(fresh):"v"(pp):"memory"); }
      WAITV(9); BAR();
      asm volatile("s_waitcnt vmcnt(0)":"+v"(fresh)::"memory");
      BAR();
      if(lw==0 && lane<32 && pj!=jj) __hip_atomic_store((gu64*)(ctl+OIN_OFF+(g8+8*(mb*8+pj))*256+(jj*4+(lane&3))*8),fresh,RLX_AGENT);
    }
    LST(25);
    asm volatile(".Lpf_end:");
    return;
  }
  const unsigned lb=(unsigned)(size_t)(LAS char*)smem;
  const int g=lane>>4, i16=lane&15, q=i16>>2, p=lane&3;
  const int wm=w>>1, wn=w&1;
  const int pid = (lane<8) ? (g8+8*(mb*8+lane)) : ((br*4+(lane&3))+8*j);
  unsigned xa[4], wc[2][2];
  {
    const int s=(i16>>1)&7;
    #pragma unroll
    for(int kk=0;kk<2;++kk)
    #pragma unroll
    for(int hf=0;hf<2;++hf) xa[kk*2+hf]=lb+128*(wm*64+32*(wn^hf)+i16)+16*((4*kk+g)^s);
  }
  #pragma unroll
  for(int c1=0;c1<2;++c1)
  #pragma unroll
  for(int t=0;t<2;++t){ int X=((t^g)&1)<<1; wc[c1][t]=lb+16384+2048*g+64*(4*t+q)+16*((2*c1+(p>>1))^X)+8*(p&1)+1024*wn; }
  f32x4 acc[4][4];
  #pragma unroll
  for(int m=0;m<4;++m)
  #pragma unroll
  for(int n=0;n<4;++n) acc[m][n]=(f32x4){0,0,0,0};
  unsigned sink=0;
  if(w==1){
    u64 pc; int len;
    asm volatile("s_getpc_b64 %0\n.Lpf_here:\n\ts_mov_b32 %1, .Lpf_end-.Lpf_here":"=s"(pc),"=s"(len));
    #pragma unroll
    for(int i=0;i<2;++i){
      const int off=(i*64+lane)*128;
      if(off<len){ const GAS char* a=(const GAS char*)(pc+off); asm volatile("global_load_dword %0, %1, off":"+v"(sink):"v"(a):"memory"); }
    }
  }
  if(w==0){
    gu64* rp_=(gu64*)(ctl+R_OFF+b*128);
    u64 old=__hip_atomic_load(rp_,RLX_AGENT);
    u64 n=old*0x9E3779B97F4A7C15ull+0x632BE59BD9B4E019ull;
    if(lane==0){ __hip_atomic_store(rp_,n,RLX_AGENT); *(volatile LAS u64*)((LAS char*)smem+NLDS)=n; }
    WAITL(0);
  }
  BAR();
  CST(30);
  u64 pn;
  { gu64* pp=(gu64*)(ctl+R_OFF+pid*128); asm volatile("global_load_dwordx2 %0, %1, off sc1":"=v"(pn):"v"(pp):"memory"); }
  gemm_loop<0>(acc,xa,wc,nullptr,0,false);
  asm volatile("s_waitcnt vmcnt(0)":"+v"(pn),"+v"(sink)::"memory");
  CST(31);
  v2u Kf[2][2][2][2];
  {
    const int f=((i16&3)<<2)|((i16>>2)&3);
    #pragma unroll
    for(int l=1;l>=0;--l)
    #pragma unroll
    for(int mm=0;mm<2;++mm)
    #pragma unroll
    for(int t=0;t<2;++t){
      unsigned a=lb+wm*SLOT+(wn^l)*8192+256*i16+16*(((2*wn+mm)*4+2*t+(g>>1))^f)+8*(g&1);
      #pragma unroll
      for(int kb=0;kb<2;++kb) RD64(Kf[l][kb][mm][t],a,kb*4096);
    }
  }
  #pragma unroll
  for(int m=0;m<4;++m) FENCE4("",acc[m][0],acc[m][1],acc[m][2],acc[m][3]);
  v4u QB[4][2];
  #pragma unroll
  for(int n=3;n>=0;--n)
  #pragma unroll
  for(int mm=0;mm<2;++mm){
    QB[n][mm]=(v4u){pk(acc[2*mm][n][0],acc[2*mm][n][1]),pk(acc[2*mm][n][2],acc[2*mm][n][3]),pk(acc[2*mm+1][n][0],acc[2*mm+1][n][1]),pk(acc[2*mm+1][n][2],acc[2*mm+1][n][3])};
  }
  WAITL(0);
  FENCE4("",QB[0][0],QB[0][1],QB[1][0],QB[1][1]);
  FENCE4("s_nop 1",QB[2][0],QB[2][1],QB[3][0],QB[3][1]);
  f32x4 S[2][2][2];
  #pragma unroll
  for(int kb=0;kb<2;++kb)
  #pragma unroll
  for(int qb=0;qb<2;++qb) M16Z(S[1][kb][qb],cat(Kf[1][kb][0][0],Kf[1][kb][0][1]),QB[2+qb][0]);
  #pragma unroll
  for(int kb=0;kb<2;++kb)
  #pragma unroll
  for(int qb=0;qb<2;++qb) M16(S[1][kb][qb],cat(Kf[1][kb][1][0],Kf[1][kb][1][1]),QB[2+qb][1]);
  #pragma unroll
  for(int kb=0;kb<2;++kb)
  #pragma unroll
  for(int qb=0;qb<2;++qb) M16Z(S[0][kb][qb],cat(Kf[0][kb][0][0],Kf[0][kb][0][1]),QB[qb][0]);
  FENCE4("s_nop 3",S[1][0][0],S[1][0][1],S[1][1][0],S[1][1][1]);
  {
    const unsigned sc=lb+SCR+w*4096+lane*16;
    #pragma unroll
    for(int kb=0;kb<2;++kb)
    #pragma unroll
    for(int qb=0;qb<2;++qb) WR128(sc,(kb*2+qb)*1024,S[1][kb][qb]);
  }
  #pragma unroll
  for(int kb=0;kb<2;++kb)
  #pragma unroll
  for(int qb=0;qb<2;++qb) M16(S[0][kb][qb],cat(Kf[0][kb][1][0],Kf[0][kb][1][1]),QB[qb][1]);
  WAITL(0);
  BAR();
  CST(32);
  f32x4 T[2][2];
  v2u Vf[8][2];
  {
    const unsigned sc=lb+SCR+(w^1)*4096+lane*16;
    #pragma unroll
    for(int kb=0;kb<2;++kb)
    #pragma unroll
    for(int qb=0;qb<2;++qb) RD128(T[kb][qb],sc,(kb*2+qb)*1024);
    const int f=(q<<2)|g;
    #pragma unroll
    for(int u=0;u<2;++u){
      unsigned a=lb+(2+wm)*SLOT+wn*8192+256*(4*g+q)+16*((4*u+p)^f);
      #pragma unroll
      for(int hf=0;hf<2;++hf)
      #pragma unroll
      for(int t=0;t<2;++t) RDTR(Vf[2*u+hf][t],a,t*4096+hf*8);
    }
    WAITL(8);
    FENCE4("",T[0][0],T[0][1],T[1][0],T[1][1]);
    FENCE4("",S[0][0][0],S[0][0][1],S[0][1][0],S[0][1][1]);
    #pragma unroll
    for(int u=2;u<4;++u){
      unsigned a=lb+(2+wm)*SLOT+wn*8192+256*(4*g+q)+16*((4*u+p)^f);
      #pragma unroll
      for(int hf=0;hf<2;++hf)
      #pragma unroll
      for(int t=0;t<2;++t) RDTR(Vf[2*u+hf][t],a,t*4096+hf*8);
    }
    #pragma unroll
    for(int kb=0;kb<2;++kb)
    #pragma unroll
    for(int qb=0;qb<2;++qb) T[kb][qb]+=S[0][kb][qb];
  }
  v4u PB[2];
  constexpr float SCL=0.08838834764831845f*1.4426950408889634f;
  {
    float mx[2], sum[2], e[2][8];
    #pragma unroll
    for(int qb=0;qb<2;++qb)
      mx[qb]=fmaxf(fmaxf(fmaxf(T[0][qb][0],T[0][qb][1]),fmaxf(T[0][qb][2],T[0][qb][3])),fmaxf(fmaxf(T[1][qb][0],T[1][qb][1]),fmaxf(T[1][qb][2],T[1][qb][3])));
    #pragma unroll
    for(int qb=0;qb<2;++qb){ auto r=__builtin_amdgcn_permlane16_swap(__float_as_uint(mx[qb]),__float_as_uint(mx[qb]),false,false); mx[qb]=fmaxf(__uint_as_float(r[0]),__uint_as_float(r[1])); }
    #pragma unroll
    for(int qb=0;qb<2;++qb){ auto r=__builtin_amdgcn_permlane32_swap(__float_as_uint(mx[qb]),__float_as_uint(mx[qb]),false,false); mx[qb]=fmaxf(__uint_as_float(r[0]),__uint_as_float(r[1])); }
    #pragma unroll
    for(int qb=0;qb<2;++qb){
      const float nm=-mx[qb]*SCL; float s4[2];
      #pragma unroll
      for(int kb=0;kb<2;++kb){
        #pragma unroll
        for(int c=0;c<4;++c) e[qb][kb*4+c]=__builtin_amdgcn_exp2f(__builtin_fmaf(T[kb][qb][c],SCL,nm));
        s4[kb]=(e[qb][kb*4]+e[qb][kb*4+1])+(e[qb][kb*4+2]+e[qb][kb*4+3]);
      }
      sum[qb]=s4[0]+s4[1];
    }
    #pragma unroll
    for(int qb=0;qb<2;++qb){ auto r=__builtin_amdgcn_permlane16_swap(__float_as_uint(sum[qb]),__float_as_uint(sum[qb]),false,false); sum[qb]=__uint_as_float(r[0])+__uint_as_float(r[1]); }
    #pragma unroll
    for(int qb=0;qb<2;++qb){ auto r=__builtin_amdgcn_permlane32_swap(__float_as_uint(sum[qb]),__float_as_uint(sum[qb]),false,false); sum[qb]=__uint_as_float(r[0])+__uint_as_float(r[1]); }
    #pragma unroll
    for(int qb=0;qb<2;++qb){
      const float inv=__builtin_amdgcn_rcpf(sum[qb]);
      PB[qb]=(v4u){pk(e[qb][0]*inv,e[qb][1]*inv),pk(e[qb][2]*inv,e[qb][3]*inv),pk(e[qb][4]*inv,e[qb][5]*inv),pk(e[qb][6]*inv,e[qb][7]*inv)};
    }
  }
  WAITL(0);
  asm volatile("s_nop 1":"+v"(PB[0]),"+v"(PB[1]));
  f32x4 O[8][2];
  #pragma unroll
  for(int m=0;m<8;++m)
  #pragma unroll
  for(int qb=0;qb<2;++qb) M16Z(O[m][qb],cat(Vf[m][0],Vf[m][1]),PB[qb]);
  {
    const __amdgpu_buffer_rsrc_t rs=__builtin_amdgcn_make_buffer_rsrc((void*)(P.ws+O_OFF+((size_t)(br*4+mb)*64+2*head)*16384),0,2*16384,0x00020000);
    const int bb=2*wm+wn;
    const unsigned oa=lb+bb*4096+lane*16;
    #pragma unroll
    for(int mm=0;mm<4;++mm){
      if(mm==0) FENCE4("",O[0][0],O[0][1],O[1][0],O[1][1]);
      if(mm==1) FENCE4("",O[2][0],O[2][1],O[3][0],O[3][1]);
      if(mm==2) FENCE4("",O[4][0],O[4][1],O[5][0],O[5][1]);
      if(mm==3) FENCE4("",O[6][0],O[6][1],O[7][0],O[7][1]);
      #pragma unroll
      for(int qb=0;qb<2;++qb){
        v4u o=(v4u){pk(O[2*mm][qb][0],O[2*mm][qb][1]),pk(O[2*mm][qb][2],O[2*mm][qb][3]),pk(O[2*mm+1][qb][0],O[2*mm+1][qb][1]),pk(O[2*mm+1][qb][2],O[2*mm+1][qb][3])};
        __builtin_amdgcn_raw_buffer_store_b128(o,rs,bb*4096+lane*16,(mm>>1)*16384+qb*2048+(mm&1)*1024,SC1);
        WR128(oa,(mm>>1)*SLOT+qb*2048+(mm&1)*1024,o);
      }
    }
  }
  #pragma unroll
  for(int n=0;n<4;++n) xa[n]=lb+lane*16+wm*8192+((n^oct)*2048);
  #pragma unroll
  for(int c1=0;c1<2;++c1)
  #pragma unroll
  for(int t=0;t<2;++t){ int X=((t^g)&1)<<1; wc[c1][t]=lb+16384+2048*g+64*(4*t+q)+16*((2*c1+(p>>1))^X)+8*(p&1)+1024*wn; }
  #pragma unroll
  for(int m=0;m<4;++m)
  #pragma unroll
  for(int n=0;n<4;++n) acc[m][n]=(f32x4){0,0,0,0};
  WAITL(0);
  WAITV(0);
  if((lane<8)&&(lane!=jj)) __hip_atomic_store((gu64*)(ctl+OIN_OFF+pid*256+(jj*4+w)*8),pn,RLX_AGENT);
  CST(33);
  BAR();
  CST(34);
  gemm_loop<1>(acc,xa,wc,(gu64*)(ctl+OIN_OFF+pid*256+(jj*4+w)*8),pn,(lane<8)&&(lane!=jj));
  CST(35);
  FENCE4("s_nop 7",acc[3][0],acc[3][1],acc[3][2],acc[3][3]);
  #pragma unroll
  for(int m=0;m<3;++m) FENCE4("",acc[m][0],acc[m][1],acc[m][2],acc[m][3]);
  {
    const int rgrp=(br*4+mb)*8+jj;
    const __amdgpu_buffer_rsrc_t rp=__builtin_amdgcn_make_buffer_rsrc((void*)(P.ws+PART_OFF+(size_t)rgrp*(4*4*4*2048)),0,4*4*4*2048,0x00020000);
    #pragma unroll
    for(int n=1;n<4;++n){
      #pragma unroll
      for(int m=0;m<4;m+=2){
        v4u o=(v4u){pk(acc[m][n][0],acc[m][n][1]),pk(acc[m][n][2],acc[m][n][3]),pk(acc[m+1][n][0],acc[m+1][n][1]),pk(acc[m+1][n][2],acc[m+1][n][3])};
        __builtin_amdgcn_raw_buffer_store_b128(o,rp,lane*16,(((n^oct)*4+oct)*4+w)*2048+m*512,SC1);
      }
    }
    WAITV(0);
    const bool rpart=(lane>=8)&&(lane<12)&&((lane&3)!=oct);
    gu64* rflag=(gu64*)(ctl+RIN_OFF+pid*128+(oct*4+w)*8);
    if(rpart) __hip_atomic_store(rflag,pn,RLX_AGENT);
    CST(36);
    const u64 myn=*(volatile LAS u64*)((LAS char*)smem+NLDS);
    poll_eq((gu64*)(ctl+RIN_OFF+b*128+((lane&3)*4+w)*8),myn,(lane<4)&&(lane!=oct));
    CST(37);
    const u64 fresh=__hip_atomic_load((gu64*)(ctl+R_OFF+pid*128),RLX_AGENT);
    v4u in[3][2];
    #pragma unroll
    for(int e=1;e<4;++e){
      #pragma unroll
      for(int m=0;m<2;++m) in[e-1][m]=__builtin_amdgcn_raw_buffer_load_b128(rp,lane*16,((oct*4+(e^oct))*4+w)*2048+m*1024,SC1);
    }
    #pragma unroll
    for(int e=0;e<3;++e){
      #pragma unroll
      for(int m=0;m<2;++m){
        acc[2*m][0][0]+=bflo(in[e][m][0]); acc[2*m][0][1]+=bfhi(in[e][m][0]); acc[2*m][0][2]+=bflo(in[e][m][1]); acc[2*m][0][3]+=bfhi(in[e][m][1]);
        acc[2*m+1][0][0]+=bflo(in[e][m][2]); acc[2*m+1][0][1]+=bfhi(in[e][m][2]); acc[2*m+1][0][2]+=bflo(in[e][m][3]); acc[2*m+1][0][3]+=bfhi(in[e][m][3]);
      }
    }
    if(rpart) __hip_atomic_store(rflag,fresh,RLX_AGENT);
    u16* op=P.out+(size_t)(mb*128+wm*64+16*oct+i16)*2048+br*1024+jj*128+wn*64+4*g;
    #pragma unroll
    for(int m=0;m<4;++m) *(GAS v2u*)(op+16*m)=(v2u){pk(acc[m][0][0],acc[m][0][1]),pk(acc[m][0][2],acc[m][0][3])};
    CST(38);
  }
}

extern "C" void kernel_launch(void* const* d_in, const int* in_sizes, int n_in,
                              void* d_out, int out_size, void* d_ws, size_t ws_size,
                              hipStream_t stream) {
  Ptrs P;
  P.x0=(const u16*)d_in[0]; P.k0=(const u16*)d_in[1]; P.v0=(const u16*)d_in[2]; P.wq0=(const u16*)d_in[3]; P.wo0=(const u16*)d_in[4];
  P.x1=(const u16*)d_in[5]; P.k1=(const u16*)d_in[6]; P.v1=(const u16*)d_in[7]; P.wq1=(const u16*)d_in[8]; P.wo1=(const u16*)d_in[9];
  P.out=(u16*)d_out; P.ws=(char*)d_ws;
  const int lds=LDS_BYTES;
  (void)hipFuncSetAttribute((const void*)k_fused,hipFuncAttributeMaxDynamicSharedMemorySize,lds);
  k_fused<<<256,512,lds,stream>>>(P);
}
```

```cpp
#define KVMODE 0
#define EARLYPUB 1
#define CODEPF 0
#define TLBW 1
#include <hip/hip_runtime.h>
#include <hip/hip_bf16.h>
typedef unsigned v4u __attribute__((ext_vector_type(4)));
typedef unsigned v2u __attribute__((ext_vector_type(2)));
typedef float f32x4 __attribute__((ext_vector_type(4)));
typedef unsigned short u16;
typedef unsigned long long u64;
#define GAS __attribute__((address_space(1)))
#define LAS __attribute__((address_space(3)))
typedef GAS u64 gu64;
#define M16(c,a,b) asm volatile("v_mfma_f32_16x16x32_bf16 %0, %1, %2, %0":"+v"(c):"v"(a),"v"(b))
#define M16Z(c,a,b) asm volatile("v_mfma_f32_16x16x32_bf16 %0, %1, %2, 0":"=v"(c):"v"(a),"v"(b))
#define FENCE4(n,a,b,c,d) asm volatile(n:"+v"(a),"+v"(b),"+v"(c),"+v"(d))
#define RD128(d,addr,off) asm volatile("ds_read_b128 %0, %1 offset:%2":"=v"(d):"v"(addr),"i"(off))
#define RD64(d,addr,off) asm volatile("ds_read_b64 %0, %1 offset:%2":"=v"(d):"v"(addr),"i"(off))
#define RDTR(d,addr,off) asm volatile("ds_read_b64_tr_b16 %0, %1 offset:%2":"=v"(d):"v"(addr),"i"(off))
#define WR128(addr,off,d) asm volatile("ds_write_b128 %0, %1 offset:%2"::"v"(addr),"v"(d),"i"(off):"memory")
#define WAITL(n) asm volatile("s_waitcnt lgkmcnt(%0)"::"i"(n):"memory")
#define WAITV(n) asm volatile("s_waitcnt vmcnt(%0)"::"i"(n):"memory")
#define BAR() asm volatile("s_barrier":::"memory")
#define RLX_AGENT __ATOMIC_RELAXED, __HIP_MEMORY_SCOPE_AGENT
constexpr int SC1=16;
constexpr int SLOT=32768;
constexpr int SCR=4*SLOT;
constexpr int NLDS=SCR+16384;
constexpr int LDS_BYTES=NLDS+64;
constexpr size_t O_OFF=0;
constexpr size_t PART_OFF=(size_t)8<<20;
constexpr size_t CTL_OFF=(size_t)32<<20;
constexpr int R_OFF=0;
constexpr int OIN_OFF=256*128;
constexpr int RIN_OFF=OIN_OFF+256*256;
constexpr unsigned SPIN_CAP=1u<<16;

#define CST(i)
#define LCY(i)
#define LST(i)

__device__ __forceinline__ v4u cat(v2u a,v2u b){ return __builtin_shufflevector(a,b,0,1,2,3); }
__device__ __forceinline__ unsigned pk(float a,float b){ unsigned r; asm("v_cvt_pk_bf16_f32 %0, %1, %2":"=v"(r):"v"(a),"v"(b)); return r; }
__device__ __forceinline__ float bflo(unsigned u){ return __uint_as_float(u<<16); }
__device__ __forceinline__ float bfhi(unsigned u){ return __uint_as_float(u&0xffff0000u); }

#define DMA(rs,dst,voff,soff,u,aux) do{ if((u)==0) __builtin_amdgcn_raw_ptr_buffer_load_lds(rs,(LAS void*)(dst),16,voff,soff,0,aux); \
  else if((u)==1) __builtin_amdgcn_raw_ptr_buffer_load_lds(rs,(LAS void*)(dst),16,voff,soff,1024,aux); \
  else if((u)==2) __builtin_amdgcn_raw_ptr_buffer_load_lds(rs,(LAS void*)(dst),16,voff,soff,2048,aux); \
  else __builtin_amdgcn_raw_ptr_buffer_load_lds(rs,(LAS void*)(dst),16,voff,soff,3072,aux); }while(0)
struct Ptrs { const u16 *x0,*x1,*k0,*k1,*v0,*v1,*wq0,*wq1,*wo0,*wo1; u16* out; char* ws; };

template<int PH> __device__ __forceinline__ void rdB(v4u& d, unsigned (&xa)[4], int n, int kk){
  if(PH==0){ if(n&1) RD128(d,xa[kk*2+(n>>1)],2048); else RD128(d,xa[kk*2+(n>>1)],0); }
  else { if(kk) RD128(d,xa[n],1024); else RD128(d,xa[n],0); }
}
template<int KK> __device__ __forceinline__ void rdA(v2u& d, unsigned (&wc)[2][2], int m, int t){
  if(m>>1) RDTR(d,wc[m&1][t],KK*8192+512); else RDTR(d,wc[m&1][t],KK*8192);
}

template<int PH>
__device__ __forceinline__ void gemm_loop(f32x4 (&acc)[4][4], unsigned (&xa)[4], unsigned (&wc)[2][2], gu64* flag, u64 tag, bool doflag){
  v2u A[2][4][2]; v4u B[2][4];
  #pragma unroll
  for(int n=0;n<4;++n) rdB<PH>(B[0][n],xa,n,0);
  #pragma unroll
  for(int m=0;m<4;++m){ rdA<0>(A[0][m][0],wc,m,0); rdA<0>(A[0][m][1],wc,m,1); }
  #pragma unroll 1
  for(int s=0;s<16;++s){
    #pragma unroll
    for(int m=0;m<4;++m){
      if(m==0) WAITL(6); else if(m==1) WAITL(8); else if(m==2) WAITL(10); else WAITL(12);
      #pragma unroll
      for(int n=0;n<4;++n){
        M16(acc[m][n],cat(A[0][m][0],A[0][m][1]),B[0][n]);
        if(m==0) rdB<PH>(B[1][n],xa,n,1);
        else if(m<3){ int r=(m-1)*4+n; rdA<1>(A[1][r>>1][r&1],wc,r>>1,r&1); }
      }
    }
    WAITL(6);
    #pragma unroll
    for(int n=0;n<4;++n) M16(acc[0][n],cat(A[1][0][0],A[1][0][1]),B[1][n]);
    WAITL(0);
    BAR();
    {
      unsigned d = ((s&3)==3) ? (unsigned)(-3*SLOT) : (unsigned)SLOT;
      xa[0]+=d; xa[1]+=d; xa[2]+=d; xa[3]+=d; wc[0][0]+=d; wc[0][1]+=d; wc[1][0]+=d; wc[1][1]+=d;
    }
    #pragma unroll
    for(int m=1;m<4;++m){
      #pragma unroll
      for(int n=0;n<4;++n){
        M16(acc[m][n],cat(A[1][m][0],A[1][m][1]),B[1][n]);
        if(m==1) rdB<PH>(B[0][n],xa,n,0);
        else { int r=(m-2)*4+n; rdA<0>(A[0][r>>1][r&1],wc,r>>1,r&1); }
      }
    }
  }
  WAITL(0);
}

__device__ __forceinline__ bool poll_eq(gu64* p, u64 want, bool active){
  for(unsigned spins=0;spins<SPIN_CAP;++spins){
    u64 v = active ? __hip_atomic_load(p,RLX_AGENT) : want;
    if(__all(v==want)) return true;
    __builtin_amdgcn_s_sleep(1);
  }
  return false;
}

__global__ __launch_bounds__(512) void k_fused(Ptrs P){
  extern __shared__ __attribute__((aligned(16))) char smem[];
  const int lane=threadIdx.x&63, w=__builtin_amdgcn_readfirstlane(threadIdx.x>>6);
  const int b=blockIdx.x, g8=b&7, j=b>>3;
  const int br=g8>>2, oct=g8&3, jj=j&7, mb=j>>3;
  const int head=oct*8+jj;
  GAS char* ctl=(GAS char*)(P.ws+CTL_OFF);
  if(w>=4){
    const int lw=w-4;
    LAS char* L=(LAS char*)smem;
    const unsigned lb=(unsigned)(size_t)L;
    LST(0);
    const int rot=(4*mb+2*jj);
    const __amdgpu_buffer_rsrc_t rx=__builtin_amdgcn_make_buffer_rsrc((void*)((br?P.x1:P.x0)+(size_t)mb*128*1024),0,128*2048,0x00020000);
    const __amdgpu_buffer_rsrc_t rw=__builtin_amdgcn_make_buffer_rsrc((void*)((br?P.wq1:P.wq0)+head*128-2048),0,1024*8192-head*256+4096,0x00020000);
    int vx[4], vw[4];
    #pragma unroll
    for(int u=0;u<4;++u){
      vx[u]=(lane>>3)*2048 + 16*((lane&7) ^ (((lane>>4)&3)|((u&1)<<2))) + u*(8*2048-1024);
      const int X=((((lane>>4)&1)^(u>>1))<<1);
      vw[u]=((lane>>2)&7)*8192 + (lane>>5)*64 + 16*((lane&3)^X) + (u>>1)*(8*8192) + (u&1)*128 - u*1024 + 4096;
    }
    auto issue1=[&](int s){
      const int st=(s+rot)&15; LAS char* d=L+(s&3)*SLOT+lw*4096;
      const int sx=lw*(4*8*2048)+st*128, sw=(st*64+lw*16)*8192;
      #pragma unroll
      for(int u=0;u<4;++u) DMA(rx,d,vx[u],sx,u,0);
      #pragma unroll
      for(int u=0;u<4;++u) DMA(rw,d+16384,vw[u],sw,u,0);
    };
    issue1(0); issue1(1);
    WAITV(8); BAR(); LST(1); LCY(43);
    issue1(2); issue1(3);
    const __amdgpu_buffer_rsrc_t rk=__builtin_amdgcn_make_buffer_rsrc((void*)((br?P.k1:P.k0)+(size_t)(mb*4*32+head)*4096),0,3*32*8192+8192,0x00020000);
    const __amdgpu_buffer_rsrc_t rv=__builtin_amdgcn_make_buffer_rsrc((void*)((br?P.v1:P.v0)+(size_t)(mb*4*32+head)*4096),0,3*32*8192+8192,0x00020000);
    const int vk=(lane>>4)*256 + 16*((lane&15) ^ (((lane>>4)<<2)|lw));
    v4u Kr[8], Vr[8];
    #pragma unroll
    for(int u=0;u<8;++u){ Kr[u]=(v4u){0,0,0,0}; Vr[u]=(v4u){0,0,0,0}; }
    const int rK=4+(j&3), rV=rK+4;
    int c_st=(4+rot)&15; unsigned c_d=lb+lw*4096;
    int c_sx=lw*(4*8*2048)+c_st*128, c_sw=(c_st*64+lw*16)*8192;
    #pragma unroll 1
    for(int s=1;s<13;++s){
      WAITV(16); BAR();
      #pragma unroll
      for(int u=0;u<4;++u) DMA(rx,(LAS char*)(size_t)c_d,vx[u],c_sx,u,0);
      __builtin_amdgcn_sched_barrier(0);
      const unsigned w_d=c_d+16384; const int w_s=c_sw;
      c_st=(c_st+1)&15; c_d=lb+lw*4096+((s+4)&3)*SLOT;
      c_sx=lw*(4*8*2048)+c_st*128; c_sw=(c_st*64+lw*16)*8192;
      asm volatile("":"+s"(c_d),"+s"(c_sx),"+s"(c_sw));
      __builtin_amdgcn_sched_barrier(0);
      #pragma unroll
      for(int u=0;u<4;++u) DMA(rw,(LAS char*)(size_t)w_d,vw[u],w_s,u,0);
      __builtin_amdgcn_sched_barrier(0);
      if(s==rK){
        #pragma unroll
        for(int u=0;u<8;++u){ int so=(u>>1)*32*8192+(lw+4*(u&1))*1024; asm volatile("buffer_load_dwordx4 %0, %1, %2, %3 offen nt":"+v"(Kr[u]):"v"(vk),"s"(rk),"s"(so):"memory"); }
      }
      if(s==rV){
        #pragma unroll
        for(int u=0;u<8;++u){ int so=(u>>1)*32*8192+(lw+4*(u&1))*1024; asm volatile("buffer_load_dwordx4 %0, %1, %2, %3 offen nt":"+v"(Vr[u]):"v"(vk),"s"(rv),"s"(so):"memory"); }
      }
    }
    LST(13); LCY(44);
    const __amdgpu_buffer_rsrc_t rwo=__builtin_amdgcn_make_buffer_rsrc((void*)((br?P.wo1:P.wo0)+(size_t)oct*1024*1024+jj*128-2048),0,1024*2048-jj*256+4096,0x00020000);
    const __amdgpu_buffer_rsrc_t ro=__builtin_amdgcn_make_buffer_rsrc((void*)(P.ws+O_OFF+((size_t)(br*4+mb)*64+oct*16)*16384),0,16*16384,0x00020000);
    int vw2[4];
    #pragma unroll
    for(int u=0;u<4;++u){
      const int X=((((lane>>4)&1)^(u>>1))<<1);
      vw2[u]=((lane>>2)&7)*2048 + (lane>>5)*64 + 16*((lane&3)^X) + (u>>1)*(8*2048) + (u&1)*128 - u*1024 + 4096;
    }
    auto hd2=[&](int r)->int{
      int t=((r-2)>>1)+2*mb; if(t>=7) t-=7; if(t>=7) t-=7;
      return (jj+1+t)&7;
    };
    auto st2=[&](int r)->int{ return r<2 ? 2*jj+r : 2*hd2(r)+(r&1); };
    auto issue_wo=[&](int r){
      LAS char* d=L+(r&3)*SLOT+16384+lw*4096; const int so=(st2(r)*64+lw*16)*2048;
      #pragma unroll
      for(int u=0;u<4;++u) DMA(rwo,d,vw2[u],so,u,0);
    };
    auto issue_o=[&](int r){
      LAS char* d=L+(r&3)*SLOT+lw*4096; const int so=st2(r)*16384+lw*4096;
      #pragma unroll
      for(int u=0;u<4;++u) DMA(ro,d,lane*16,so,u,SC1);
    };
    const unsigned kva=lb+lw*1024+lane*16;
    WAITV(16); BAR(); LST(14);
    #pragma unroll
    for(int u=0;u<4;++u) WR128(kva,(u>>1)*8192+(u&1)*4096,Kr[u]);
    issue_wo(0);
    WAITV(12); BAR();
    #pragma unroll
    for(int u=4;u<8;++u) WR128(kva,SLOT+((u-4)>>1)*8192+(u&1)*4096,Kr[u]);
    issue_wo(1);
    WAITV(8); WAITL(0); BAR();
    #pragma unroll
    for(int u=0;u<4;++u) WR128(kva+2*SLOT,(u>>1)*8192+(u&1)*4096,Vr[u]);
    issue_wo(2);
    WAITL(0); BAR(); LST(17);
    #pragma unroll
    for(int u=4;u<8;++u) WR128(kva+3*SLOT,((u-4)>>1)*8192+(u&1)*4096,Vr[u]);
    WAITL(0); BAR(); LST(18);
    issue_wo(3);
    WAITV(12); BAR(); LST(19);
    WAITV(8); BAR(); LST(20);
    const u64 myn=*(volatile LAS u64*)(L+NLDS);
    gu64* inbox=(gu64*)(ctl+OIN_OFF+b*256+(lane&31)*8);
    unsigned ready=0xFu<<(4*jj);
    u64 pv=~myn;
    auto need_head=[&](int h){
      asm volatile("":"+v"(pv));
      ready|=(unsigned)__ballot(pv==myn);
      for(unsigned spins=0; ((ready>>(4*h))&15u)!=15u && spins<SPIN_CAP; ++spins){
        u64 v=__hip_atomic_load(inbox,RLX_AGENT);
        ready|=(unsigned)__ballot(v==myn);
        if(((ready>>(4*h))&15u)!=15u) __builtin_amdgcn_s_sleep(1);
      }
      if(ready!=0xFFFFFFFFu) asm volatile("global_load_dwordx2 %0, %1, off sc1":"+v"(pv):"v"(inbox):"memory");
    };
    need_head(hd2(2));
    LST(21);
    issue_o(2); issue_o(3); issue_wo(4);
    WAITV(8); BAR(); LST(22);
    need_head(hd2(4));
    issue_o(4); issue_o(5); issue_wo(5);
    WAITV(12); BAR(); LST(23); LCY(41);
    need_head(hd2(6));
    int n_st=st2(6); unsigned n_d=lb+lw*4096+(6&3)*SLOT;
    #pragma unroll 1
    for(int r=4;r<14;++r){
      const int so_o=n_st*16384+lw*4096, so_w=(n_st*64+lw*16)*2048; const unsigned d_o=n_d;
      #pragma unroll
      for(int u=0;u<4;++u) DMA(ro,(LAS char*)(size_t)d_o,lane*16,so_o,u,SC1);
      __builtin_amdgcn_sched_barrier(0);
      #pragma unroll
      for(int u=0;u<2;++u) DMA(rwo,(LAS char*)(size_t)(d_o+16384),vw2[u],so_w,u,0);
      __builtin_amdgcn_sched_barrier(0);
      if(r<13){
        n_st=st2(r+3); n_d=lb+lw*4096+((r+3)&3)*SLOT;
        if(r&1){ asm volatile("s_waitcnt vmcnt(14)":"+v"(pv)::"memory"); need_head(n_st>>1); }
      }
      asm volatile("":"+s"(n_st),"+s"(n_d));
      __builtin_amdgcn_sched_barrier(0);
      #pragma unroll
      for(int u=2;u<4;++u) DMA(rwo,(LAS char*)(size_t)(d_o+16384),vw2[u],so_w,u,0);
      __builtin_amdgcn_sched_barrier(0);
      WAITV(16); BAR();
    }
    LST(24); LCY(42);
    {
      const int pj=(lane>>2)&7;
      u64 fresh;
      { gu64* pp=(gu64*)(ctl+R_OFF+(g8+8*(mb*8+pj))*128); asm volatile("global_load_dwordx2 %0, %1, off sc1":"=v"(fresh):"v"(pp):"memory"); }
      WAITV(9); BAR();
      asm volatile("s_waitcnt vmcnt(0)":"+v"(fresh)::"memory");
      BAR();
      if(lw==0 && lane<32 && pj!=jj) __hip_atomic_store((gu64*)(ctl+OIN_OFF+(g8+8*(mb*8+pj))*256+(jj*4+(lane&3))*8),fresh,RLX_AGENT);
    }
    LST(25);
    return;
  }
  const unsigned lb=(unsigned)(size_t)(LAS char*)smem;
  const int g=lane>>4, i16=lane&15, q=i16>>2, p=lane&3;
  const int wm=w>>1, wn=w&1;
  const int pid = (lane<8) ? (g8+8*(mb*8+lane)) : ((br*4+(lane&3))+8*j);
  unsigned xa[4], wc[2][2];
  {
    const int s=(i16>>1)&7;
    #pragma unroll
    for(int kk=0;kk<2;++kk)
    #pragma unroll
    for(int hf=0;hf<2;++hf) xa[kk*2+hf]=lb+128*(wm*64+32*(wn^hf)+i16)+16*((4*kk+g)^s);
  }
  #pragma unroll
  for(int c1=0;c1<2;++c1)
  #pragma unroll
  for(int t=0;t<2;++t){ int X=((t^g)&1)<<1; wc[c1][t]=lb+16384+2048*g+64*(4*t+q)+16*((2*c1+(p>>1))^X)+8*(p&1)+1024*wn; }
  f32x4 acc[4][4];
  #pragma unroll
  for(int m=0;m<4;++m)
  #pragma unroll
  for(int n=0;n<4;++n) acc[m][n]=(f32x4){0,0,0,0};
  unsigned tsink=0;
  if(w==2){
    const GAS char* a;
    if(lane==0) a=(const GAS char*)(P.ws+O_OFF+((size_t)(br*4+mb)*64+oct*16)*16384);
    else if(lane==1) a=(const GAS char*)(P.ws+PART_OFF+(size_t)((br*4+mb)*8+jj)*(4*4*4*2048));
    else if(lane==2) a=(const GAS char*)(P.out+(size_t)(mb*128)*2048);
    else if(lane==3) a=(const GAS char*)((br?P.wo1:P.wo0)+(size_t)oct*1024*1024);
    else if(lane==4) a=(const GAS char*)((br?P.k1:P.k0)+(size_t)(mb*4*32+head)*4096);
    else a=(const GAS char*)((br?P.v1:P.v0)+(size_t)(mb*4*32+head)*4096);
    if(lane<6) asm volatile("global_load_dword %0, %1, off":"+v"(tsink):"v"(a):"memory");
  }
  if(w==0){
    gu64* rp_=(gu64*)(ctl+R_OFF+b*128);
    u64 old=__hip_atomic_load(rp_,RLX_AGENT);
    u64 n=old*0x9E3779B97F4A7C15ull+0x632BE59BD9B4E019ull;
    if(lane==0){ __hip_atomic_store(rp_,n,RLX_AGENT); *(volatile LAS u64*)((LAS char*)smem+NLDS)=n; }
    WAITL(0);
  }
  BAR();
  CST(30);
  u64 pn;
  { gu64* pp=(gu64*)(ctl+R_OFF+pid*128); asm volatile("global_load_dwordx2 %0, %1, off sc1":"=v"(pn):"v"(pp):"memory"); }
  gemm_loop<0>(acc,xa,wc,nullptr,0,false);
  asm volatile("s_waitcnt vmcnt(0)":"+v"(pn),"+v"(tsink)::"memory");
  CST(31);
  v2u Kf[2][2][2][2];
  {
    const int f=((i16&3)<<2)|((i16>>2)&3);
    #pragma unroll
    for(int l=1;l>=0;--l)
    #pragma unroll
    for(int mm=0;mm<2;++mm)
    #pragma unroll
    for(int t=0;t<2;++t){
      unsigned a=lb+wm*SLOT+(wn^l)*8192+256*i16+16*(((2*wn+mm)*4+2*t+(g>>1))^f)+8*(g&1);
      #pragma unroll
      for(int kb=0;kb<2;++kb) RD64(Kf[l][kb][mm][t],a,kb*4096);
    }
  }
  #pragma unroll
  for(int m=0;m<4;++m) FENCE4("",acc[m][0],acc[m][1],acc[m][2],acc[m][3]);
  v4u QB[4][2];
  #pragma unroll
  for(int n=3;n>=0;--n)
  #pragma unroll
  for(int mm=0;mm<2;++mm){
    QB[n][mm]=(v4u){pk(acc[2*mm][n][0],acc[2*mm][n][1]),pk(acc[2*mm][n][2],acc[2*mm][n][3]),pk(acc[2*mm+1][n][0],acc[2*mm+1][n][1]),pk(acc[2*mm+1][n][2],acc[2*mm+1][n][3])};
  }
  WAITL(0);
  FENCE4("",QB[0][0],QB[0][1],QB[1][0],QB[1][1]);
  FENCE4("s_nop 1",QB[2][0],QB[2][1],QB[3][0],QB[3][1]);
  f32x4 S[2][2][2];
  #pragma unroll
  for(int kb=0;kb<2;++kb)
  #pragma unroll
  for(int qb=0;qb<2;++qb) M16Z(S[1][kb][qb],cat(Kf[1][kb][0][0],Kf[1][kb][0][1]),QB[2+qb][0]);
  #pragma unroll
  for(int kb=0;kb<2;++kb)
  #pragma unroll
  for(int qb=0;qb<2;++qb) M16(S[1][kb][qb],cat(Kf[1][kb][1][0],Kf[1][kb][1][1]),QB[2+qb][1]);
  #pragma unroll
  for(int kb=0;kb<2;++kb)
  #pragma unroll
  for(int qb=0;qb<2;++qb) M16Z(S[0][kb][qb],cat(Kf[0][kb][0][0],Kf[0][kb][0][1]),QB[qb][0]);
  FENCE4("s_nop 3",S[1][0][0],S[1][0][1],S[1][1][0],S[1][1][1]);
  {
    const unsigned sc=lb+SCR+w*4096+lane*16;
    #pragma unroll
    for(int kb=0;kb<2;++kb)
    #pragma unroll
    for(int qb=0;qb<2;++qb) WR128(sc,(kb*2+qb)*1024,S[1][kb][qb]);
  }
  #pragma unroll
  for(int kb=0;kb<2;++kb)
  #pragma unroll
  for(int qb=0;qb<2;++qb) M16(S[0][kb][qb],cat(Kf[0][kb][1][0],Kf[0][kb][1][1]),QB[qb][1]);
  WAITL(0);
  BAR();
  CST(32);
  f32x4 T[2][2];
  v2u Vf[8][2];
  {
    const unsigned sc=lb+SCR+(w^1)*4096+lane*16;
    #pragma unroll
    for(int kb=0;kb<2;++kb)
    #pragma unroll
    for(int qb=0;qb<2;++qb) RD128(T[kb][qb],sc,(kb*2+qb)*1024);
    const int f=(q<<2)|g;
    #pragma unroll
    for(int u=0;u<2;++u){
      unsigned a=lb+(2+wm)*SLOT+wn*8192+256*(4*g+q)+16*((4*u+p)^f);
      #pragma unroll
      for(int hf=0;hf<2;++hf)
      #pragma unroll
      for(int t=0;t<2;++t) RDTR(Vf[2*u+hf][t],a,t*4096+hf*8);
    }
    WAITL(8);
    FENCE4("",T[0][0],T[0][1],T[1][0],T[1][1]);
    FENCE4("",S[0][0][0],S[0][0][1],S[0][1][0],S[0][1][1]);
    #pragma unroll
    for(int u=2;u<4;++u){
      unsigned a=lb+(2+wm)*SLOT+wn*8192+256*(4*g+q)+16*((4*u+p)^f);
      #pragma unroll
      for(int hf=0;hf<2;++hf)
      #pragma unroll
      for(int t=0;t<2;++t) RDTR(Vf[2*u+hf][t],a,t*4096+hf*8);
    }
    #pragma unroll
    for(int kb=0;kb<2;++kb)
    #pragma unroll
    for(int qb=0;qb<2;++qb) T[kb][qb]+=S[0][kb][qb];
  }
  v4u PB[2];
  constexpr float SCL=0.08838834764831845f*1.4426950408889634f;
  {
    float mx[2], sum[2], e[2][8];
    #pragma unroll
    for(int qb=0;qb<2;++qb)
      mx[qb]=fmaxf(fmaxf(fmaxf(T[0][qb][0],T[0][qb][1]),fmaxf(T[0][qb][2],T[0][qb][3])),fmaxf(fmaxf(T[1][qb][0],T[1][qb][1]),fmaxf(T[1][qb][2],T[1][qb][3])));
    #pragma unroll
    for(int qb=0;qb<2;++qb){ auto r=__builtin_amdgcn_permlane16_swap(__float_as_uint(mx[qb]),__float_as_uint(mx[qb]),false,false); mx[qb]=fmaxf(__uint_as_float(r[0]),__uint_as_float(r[1])); }
    #pragma unroll
    for(int qb=0;qb<2;++qb){ auto r=__builtin_amdgcn_permlane32_swap(__float_as_uint(mx[qb]),__float_as_uint(mx[qb]),false,false); mx[qb]=fmaxf(__uint_as_float(r[0]),__uint_as_float(r[1])); }
    #pragma unroll
    for(int qb=0;qb<2;++qb){
      const float nm=-mx[qb]*SCL; float s4[2];
      #pragma unroll
      for(int kb=0;kb<2;++kb){
        #pragma unroll
        for(int c=0;c<4;++c) e[qb][kb*4+c]=__builtin_amdgcn_exp2f(__builtin_fmaf(T[kb][qb][c],SCL,nm));
        s4[kb]=(e[qb][kb*4]+e[qb][kb*4+1])+(e[qb][kb*4+2]+e[qb][kb*4+3]);
      }
      sum[qb]=s4[0]+s4[1];
    }
    #pragma unroll
    for(int qb=0;qb<2;++qb){ auto r=__builtin_amdgcn_permlane16_swap(__float_as_uint(sum[qb]),__float_as_uint(sum[qb]),false,false); sum[qb]=__uint_as_float(r[0])+__uint_as_float(r[1]); }
    #pragma unroll
    for(int qb=0;qb<2;++qb){ auto r=__builtin_amdgcn_permlane32_swap(__float_as_uint(sum[qb]),__float_as_uint(sum[qb]),false,false); sum[qb]=__uint_as_float(r[0])+__uint_as_float(r[1]); }
    #pragma unroll
    for(int qb=0;qb<2;++qb){
      const float inv=__builtin_amdgcn_rcpf(sum[qb]);
      PB[qb]=(v4u){pk(e[qb][0]*inv,e[qb][1]*inv),pk(e[qb][2]*inv,e[qb][3]*inv),pk(e[qb][4]*inv,e[qb][5]*inv),pk(e[qb][6]*inv,e[qb][7]*inv)};
    }
  }
  WAITL(0);
  asm volatile("s_nop 1":"+v"(PB[0]),"+v"(PB[1]));
  f32x4 O[8][2];
  #pragma unroll
  for(int m=0;m<8;++m)
  #pragma unroll
  for(int qb=0;qb<2;++qb) M16Z(O[m][qb],cat(Vf[m][0],Vf[m][1]),PB[qb]);
  {
    const __amdgpu_buffer_rsrc_t rs=__builtin_amdgcn_make_buffer_rsrc((void*)(P.ws+O_OFF+((size_t)(br*4+mb)*64+2*head)*16384),0,2*16384,0x00020000);
    const int bb=2*wm+wn;
    const unsigned oa=lb+bb*4096+lane*16;
    #pragma unroll
    for(int mm=0;mm<4;++mm){
      if(mm==0) FENCE4("",O[0][0],O[0][1],O[1][0],O[1][1]);
      if(mm==1) FENCE4("",O[2][0],O[2][1],O[3][0],O[3][1]);
      if(mm==2) FENCE4("",O[4][0],O[4][1],O[5][0],O[5][1]);
      if(mm==3) FENCE4("",O[6][0],O[6][1],O[7][0],O[7][1]);
      #pragma unroll
      for(int qb=0;qb<2;++qb){
        v4u o=(v4u){pk(O[2*mm][qb][0],O[2*mm][qb][1]),pk(O[2*mm][qb][2],O[2*mm][qb][3]),pk(O[2*mm+1][qb][0],O[2*mm+1][qb][1]),pk(O[2*mm+1][qb][2],O[2*mm+1][qb][3])};
        __builtin_amdgcn_raw_buffer_store_b128(o,rs,bb*4096+lane*16,(mm>>1)*16384+qb*2048+(mm&1)*1024,SC1);
        WR128(oa,(mm>>1)*SLOT+qb*2048+(mm&1)*1024,o);
      }
    }
  }
  #pragma unroll
  for(int n=0;n<4;++n) xa[n]=lb+lane*16+wm*8192+((n^oct)*2048);
  #pragma unroll
  for(int c1=0;c1<2;++c1)
  #pragma unroll
  for(int t=0;t<2;++t){ int X=((t^g)&1)<<1; wc[c1][t]=lb+16384+2048*g+64*(4*t+q)+16*((2*c1+(p>>1))^X)+8*(p&1)+1024*wn; }
  #pragma unroll
  for(int m=0;m<4;++m)
  #pragma unroll
  for(int n=0;n<4;++n) acc[m][n]=(f32x4){0,0,0,0};
  WAITL(0);
  WAITV(0);
  if((lane<8)&&(lane!=jj)) __hip_atomic_store((gu64*)(ctl+OIN_OFF+pid*256+(jj*4+w)*8),pn,RLX_AGENT);
  CST(33);
  BAR();
  CST(34);
  gemm_loop<1>(acc,xa,wc,(gu64*)(ctl+OIN_OFF+pid*256+(jj*4+w)*8),pn,(lane<8)&&(lane!=jj));
  CST(35);
  FENCE4("s_nop 7",acc[3][0],acc[3][1],acc[3][2],acc[3][3]);
  #pragma unroll
  for(int m=0;m<3;++m) FENCE4("",acc[m][0],acc[m][1],acc[m][2],acc[m][3]);
  {
    const int rgrp=(br*4+mb)*8+jj;
    const __amdgpu_buffer_rsrc_t rp=__builtin_amdgcn_make_buffer_rsrc((void*)(P.ws+PART_OFF+(size_t)rgrp*(4*4*4*2048)),0,4*4*4*2048,0x00020000);
    #pragma unroll
    for(int n=1;n<4;++n){
      #pragma unroll
      for(int m=0;m<4;m+=2){
        v4u o=(v4u){pk(acc[m][n][0],acc[m][n][1]),pk(acc[m][n][2],acc[m][n][3]),pk(acc[m+1][n][0],acc[m+1][n][1]),pk(acc[m+1][n][2],acc[m+1][n][3])};
        __builtin_amdgcn_raw_buffer_store_b128(o,rp,lane*16,(((n^oct)*4+oct)*4+w)*2048+m*512,SC1);
      }
    }
    WAITV(0);
    const bool rpart=(lane>=8)&&(lane<12)&&((lane&3)!=oct);
    gu64* rflag=(gu64*)(ctl+RIN_OFF+pid*128+(oct*4+w)*8);
    if(rpart) __hip_atomic_store(rflag,pn,RLX_AGENT);
    CST(36);
    const u64 myn=*(volatile LAS u64*)((LAS char*)smem+NLDS);
    poll_eq((gu64*)(ctl+RIN_OFF+b*128+((lane&3)*4+w)*8),myn,(lane<4)&&(lane!=oct));
    CST(37);
    const u64 fresh=__hip_atomic_load((gu64*)(ctl+R_OFF+pid*128),RLX_AGENT);
    v4u in[3][2];
    #pragma unroll
    for(int e=1;e<4;++e){
      #pragma unroll
      for(int m=0;m<2;++m) in[e-1][m]=__builtin_amdgcn_raw_buffer_load_b128(rp,lane*16,((oct*4+(e^oct))*4+w)*2048+m*1024,SC1);
    }
    #pragma unroll
    for(int e=0;e<3;++e){
      #pragma unroll
      for(int m=0;m<2;++m){
        acc[2*m][0][0]+=bflo(in[e][m][0]); acc[2*m][0][1]+=bfhi(in[e][m][0]); acc[2*m][0][2]+=bflo(in[e][m][1]); acc[2*m][0][3]+=bfhi(in[e][m][1]);
        acc[2*m+1][0][0]+=bflo(in[e][m][2]); acc[2*m+1][0][1]+=bfhi(in[e][m][2]); acc[2*m+1][0][2]+=bflo(in[e][m][3]); acc[2*m+1][0][3]+=bfhi(in[e][m][3]);
      }
    }
    if(rpart) __hip_atomic_store(rflag,fresh,RLX_AGENT);
    u16* op=P.out+(size_t)(mb*128+wm*64+16*oct+i16)*2048+br*1024+jj*128+wn*64+4*g;
    #pragma unroll
    for(int m=0;m<4;++m) *(GAS v2u*)(op+16*m)=(v2u){pk(acc[m][0][0],acc[m][0][1]),pk(acc[m][0][2],acc[m][0][3])};
    CST(38);
  }
}

extern "C" void kernel_launch(void* const* d_in, const int* in_sizes, int n_in,
                              void* d_out, int out_size, void* d_ws, size_t ws_size,
                              hipStream_t stream) {
  Ptrs P;
  P.x0=(const u16*)d_in[0]; P.k0=(const u16*)d_in[1]; P.v0=(const u16*)d_in[2]; P.wq0=(const u16*)d_in[3]; P.wo0=(const u16*)d_in[4];
  P.x1=(const u16*)d_in[5]; P.k1=(const u16*)d_in[6]; P.v1=(const u16*)d_in[7]; P.wq1=(const u16*)d_in[8]; P.wo1=(const u16*)d_in[9];
  P.out=(u16*)d_out; P.ws=(char*)d_ws;
  const int lds=LDS_BYTES;
  (void)hipFuncSetAttribute((const void*)k_fused,hipFuncAttributeMaxDynamicSharedMemorySize,lds);
  k_fused<<<256,512,lds,stream>>>(P);
}
```

```cpp
#include <hip/hip_runtime.h>
typedef unsigned v4u __attribute__((ext_vector_type(4)));
typedef unsigned v2u __attribute__((ext_vector_type(2)));
typedef float f32x4 __attribute__((ext_vector_type(4)));
typedef unsigned short u16;
typedef unsigned long long u64;
#define GAS __attribute__((address_space(1)))
#define LAS __attribute__((address_space(3)))
typedef GAS u64 gu64;
#define M16(c,a,b) asm volatile("v_mfma_f32_16x16x32_bf16 %0, %1, %2, %0":"+v"(c):"v"(a),"v"(b))
#define M16Z(c,a,b) asm volatile("v_mfma_f32_16x16x32_bf16 %0, %1, %2, 0":"=v"(c):"v"(a),"v"(b))
#define FENCE4(n,a,b,c,d) asm volatile(n:"+v"(a),"+v"(b),"+v"(c),"+v"(d))
#define RD128(d,addr,off) asm volatile("ds_read_b128 %0, %1 offset:%2":"=v"(d):"v"(addr),"i"(off))
#define RD64(d,addr,off) asm volatile("ds_read_b64 %0, %1 offset:%2":"=v"(d):"v"(addr),"i"(off))
#define RDTR(d,addr,off) asm volatile("ds_read_b64_tr_b16 %0, %1 offset:%2":"=v"(d):"v"(addr),"i"(off))
#define WR128(addr,off,d) asm volatile("ds_write_b128 %0, %1 offset:%2"::"v"(addr),"v"(d),"i"(off):"memory")
#define WAITL(n) asm volatile("s_waitcnt lgkmcnt(%0)"::"i"(n):"memory")
#define WAITV(n) asm volatile("s_waitcnt vmcnt(%0)"::"i"(n):"memory")
#define BAR() asm volatile("s_barrier":::"memory")
#define RLX_AGENT __ATOMIC_RELAXED, __HIP_MEMORY_SCOPE_AGENT
constexpr int SC1=16;
constexpr int SLOT=32768;
constexpr int SCR=4*SLOT;
constexpr int PBUF=SCR+16384;
constexpr int NLDS=PBUF+8192;
constexpr int LDS_BYTES=PBUF+16384;
constexpr size_t P_OFF=0;
constexpr size_t PART_OFF=(size_t)8<<20;
constexpr size_t CTL_OFF=(size_t)16<<20;
constexpr int R_OFF=0;
constexpr int PIN_OFF=256*128;
constexpr int RIN_OFF=PIN_OFF+256*256;
constexpr unsigned SPIN_CAP=1u<<16;

__device__ __forceinline__ v4u cat(v2u a,v2u b){ return __builtin_shufflevector(a,b,0,1,2,3); }
__device__ __forceinline__ unsigned pk(float a,float b){ unsigned r; asm("v_cvt_pk_bf16_f32 %0, %1, %2":"=v"(r):"v"(a),"v"(b)); return r; }
__device__ __forceinline__ float bflo(unsigned u){ return __uint_as_float(u<<16); }
__device__ __forceinline__ float bfhi(unsigned u){ return __uint_as_float(u&0xffff0000u); }

#define DMA(rs,dst,voff,soff,u,aux) do{ if((u)==0) __builtin_amdgcn_raw_ptr_buffer_load_lds(rs,(LAS void*)(dst),16,voff,soff,0,aux); \
  else if((u)==1) __builtin_amdgcn_raw_ptr_buffer_load_lds(rs,(LAS void*)(dst),16,voff,soff,1024,aux); \
  else if((u)==2) __builtin_amdgcn_raw_ptr_buffer_load_lds(rs,(LAS void*)(dst),16,voff,soff,2048,aux); \
  else __builtin_amdgcn_raw_ptr_buffer_load_lds(rs,(LAS void*)(dst),16,voff,soff,3072,aux); }while(0)
struct Ptrs { const u16 *x0,*x1,*k0,*k1,*v0,*v1,*wq0,*wq1,*wo0,*wo1; u16* out; char* ws; };

template<int SP> __device__ __forceinline__ void rdB(v4u& d, unsigned (&xa)[4], int n, int kk){
  if(n&1) RD128(d,xa[kk*2+(n>>1)],SP*SLOT+2048); else RD128(d,xa[kk*2+(n>>1)],SP*SLOT);
}
template<int KK,int SP> __device__ __forceinline__ void rdA(v2u& d, unsigned (&wc)[2][2], int m, int t){
  if(m>>1) RDTR(d,wc[m&1][t],SP*SLOT+KK*8192+512); else RDTR(d,wc[m&1][t],SP*SLOT+KK*8192);
}
template<int SP>
__device__ __forceinline__ void gemm_stage(f32x4 (&acc)[4][4], v2u (&A)[2][4][2], v4u (&B)[2][4], unsigned (&xa)[4], unsigned (&wc)[2][2]){
  #pragma unroll
  for(int m=0;m<4;++m){
    if(m==0) WAITL(6); else if(m==1) WAITL(8); else if(m==2) WAITL(10); else WAITL(12);
    #pragma unroll
    for(int n=0;n<4;++n){
      M16(acc[m][n],cat(A[0][m][0],A[0][m][1]),B[0][n]);
      if(m==0) rdB<SP>(B[1][n],xa,n,1);
      else if(m<3){ int r=(m-1)*4+n; rdA<1,SP>(A[1][r>>1][r&1],wc,r>>1,r&1); }
    }
  }
  WAITL(6);
  #pragma unroll
  for(int n=0;n<4;++n) M16(acc[0][n],cat(A[1][0][0],A[1][0][1]),B[1][n]);
  WAITL(0);
  BAR();
  if(SP==1){
    xa[0]^=2*SLOT; xa[1]^=2*SLOT; xa[2]^=2*SLOT; xa[3]^=2*SLOT; wc[0][0]^=2*SLOT; wc[0][1]^=2*SLOT; wc[1][0]^=2*SLOT; wc[1][1]^=2*SLOT;
  }
  #pragma unroll
  for(int m=1;m<4;++m){
    #pragma unroll
    for(int n=0;n<4;++n){
      M16(acc[m][n],cat(A[1][m][0],A[1][m][1]),B[1][n]);
      if(m==1) rdB<SP^1>(B[0][n],xa,n,0);
      else { int r=(m-2)*4+n; rdA<0,SP^1>(A[0][r>>1][r&1],wc,r>>1,r&1); }
    }
  }
}
__device__ __forceinline__ void gemm_loop1(f32x4 (&acc)[4][4], unsigned (&xa)[4], unsigned (&wc)[2][2]){
  v2u A[2][4][2]; v4u B[2][4];
  #pragma unroll
  for(int n=0;n<4;++n) rdB<0>(B[0][n],xa,n,0);
  #pragma unroll
  for(int m=0;m<4;++m){ rdA<0,0>(A[0][m][0],wc,m,0); rdA<0,0>(A[0][m][1],wc,m,1); }
  #pragma unroll 1
  for(int s=0;s<8;++s){
    if(s==1) WAITV(0);
    gemm_stage<0>(acc,A,B,xa,wc);
    gemm_stage<1>(acc,A,B,xa,wc);
  }
  WAITL(0);
}
#define M16S(c,a,b) M16(c,b,a)
#define M16SZ(c,a,b) M16Z(c,b,a)
__device__ __forceinline__ v4u xfrag(const f32x4& lo,const f32x4& hi){ return (v4u){pk(lo[0],lo[1]),pk(lo[2],lo[3]),pk(hi[0],hi[1]),pk(hi[2],hi[3])}; }
__device__ __forceinline__ void head_step(f32x4 (&ao)[4][4], f32x4 (&aw)[4][4], v2u (&A)[2][4][2], v4u (&B)[2][4], unsigned (&xa)[4], unsigned (&wc)[2][2], unsigned (&pa)[4]){
  #pragma unroll
  for(int m=0;m<4;++m){
    if(m==0) WAITL(6); else if(m==1) WAITL(8); else if(m==2) WAITL(10); else WAITL(12);
    #pragma unroll
    for(int n=0;n<4;++n){
      M16SZ(aw[m][n],cat(A[0][m][0],A[0][m][1]),B[0][n]);
      if(m==0) rdB<0>(B[1][n],xa,n,1);
      else if(m<3){ int r=(m-1)*4+n; rdA<1,0>(A[1][r>>1][r&1],wc,r>>1,r&1); }
    }
  }
  WAITL(6);
  #pragma unroll
  for(int n=0;n<4;++n) M16S(aw[0][n],cat(A[1][0][0],A[1][0][1]),B[1][n]);
  WAITL(0);
  BAR();
  #pragma unroll
  for(int m=1;m<4;++m){
    #pragma unroll
    for(int n=0;n<4;++n){
      M16S(aw[m][n],cat(A[1][m][0],A[1][m][1]),B[1][n]);
      if(m==1) rdB<1>(B[0][n],xa,n,0);
      else { int r=(m-2)*4+n; rdA<0,1>(A[0][r>>1][r&1],wc,r>>1,r&1); }
    }
  }
  #pragma unroll
  for(int m=0;m<4;++m){
    if(m==0) WAITL(6); else if(m==1) WAITL(8); else if(m==2) WAITL(10); else WAITL(12);
    #pragma unroll
    for(int n=0;n<4;++n){
      M16S(aw[m][n],cat(A[0][m][0],A[0][m][1]),B[0][n]);
      if(m==0) rdB<1>(B[1][n],xa,n,1);
      else if(m<3){ int r=(m-1)*4+n; rdA<1,1>(A[1][r>>1][r&1],wc,r>>1,r&1); }
    }
  }
  WAITL(6);
  #pragma unroll
  for(int n=0;n<4;++n) M16S(aw[0][n],cat(A[1][0][0],A[1][0][1]),B[1][n]);
  WAITL(0);
  BAR();
  xa[0]^=2*SLOT; xa[1]^=2*SLOT; xa[2]^=2*SLOT; xa[3]^=2*SLOT; wc[0][0]^=2*SLOT; wc[0][1]^=2*SLOT; wc[1][0]^=2*SLOT; wc[1][1]^=2*SLOT;
  v4u Pf[4];
  unsigned xw[2][4][4];
#define CV(m,e) asm volatile("v_cvt_pk_bf16_f32 %0, %1, %2":"=v"(xw[(e)>>2][m][(e)&3]):"v"(aw[m][((e)>>1)][((e)&1)*2]),"v"(aw[m][((e)>>1)][((e)&1)*2+1]))
#define XF(bb,m) ((v4u){xw[bb][m][0],xw[bb][m][1],xw[bb][m][2],xw[bb][m][3]})
  #pragma unroll
  for(int k=0;k<12;++k){
    const int m=1+(k>>2), n=k&3;
    M16S(aw[m][n],cat(A[1][m][0],A[1][m][1]),B[1][n]);
    if(k==0){ RD128(Pf[0],pa[0],0); RD128(Pf[1],pa[1],0); }
    else if(k==1){ RD128(Pf[2],pa[2],0); RD128(Pf[3],pa[3],0); }
    else if(k<6){ rdB<0>(B[0][k-2],xa,k-2,0); }
    else if(k<10){ int r=k-6; rdA<0,0>(A[0][r>>1][r&1],wc,r>>1,r&1); }
    else { int r=4+(k-10)*2; rdA<0,0>(A[0][r>>1][0],wc,r>>1,0); rdA<0,0>(A[0][r>>1][1],wc,r>>1,1); }
    if(k>=2 && k<10) CV(0,k-2);
  }
  WAITL(12);
  #pragma unroll
  for(int m=0;m<4;++m){
    #pragma unroll
    for(int n=0;n<4;++n){
      M16(ao[m][n],XF(n>>1,m),Pf[n]);
      if(m<3){ CV(m+1,2*n); CV(m+1,2*n+1); }
    }
    if(m<3) asm volatile("s_nop 0");
  }
  pa[0]^=8192; pa[1]^=8192; pa[2]^=8192; pa[3]^=8192;
}

__device__ __forceinline__ bool poll_eq(gu64* p, u64 want, bool active){
  for(unsigned spins=0;spins<SPIN_CAP;++spins){
    u64 v = active ? __hip_atomic_load(p,RLX_AGENT) : want;
    if(__all(v==want)) return true;
    __builtin_amdgcn_s_sleep(1);
  }
  return false;
}

__global__ __launch_bounds__(512,2) void k_fused(Ptrs P){
  extern __shared__ __attribute__((aligned(16))) char smem[];
  asm volatile(""::"s"(P.x0),"s"(P.x1),"s"(P.k0),"s"(P.k1),"s"(P.v0),"s"(P.v1),"s"(P.wq0),"s"(P.wq1),"s"(P.wo0),"s"(P.wo1),"s"(P.out),"s"(P.ws));
  const int lane=threadIdx.x&63, w=__builtin_amdgcn_readfirstlane(threadIdx.x>>6);
  const int b=blockIdx.x, g8=b&7, j=b>>3;
  const int br=g8>>2, oct=g8&3, jj=j&7, mb=j>>3;
  const int head=oct*8+jj;
  GAS char* ctl=(GAS char*)(P.ws+CTL_OFF);
  if(__builtin_expect(w>=4,1)){
    const int lw=w-4;
    LAS char* L=(LAS char*)smem;
    const unsigned lb=(unsigned)(size_t)L;
    const int rot=(4*mb+4*oct+(oct&1));
    const __amdgpu_buffer_rsrc_t rx=__builtin_amdgcn_make_buffer_rsrc((void*)((br?P.x1:P.x0)+(size_t)mb*128*1024),0,128*2048,0x00020000);
    const __amdgpu_buffer_rsrc_t rw=__builtin_amdgcn_make_buffer_rsrc((void*)((br?P.wq1:P.wq0)+head*128-2048),0,1024*8192-head*256+4096,0x00020000);
    int vx[4], vw[4];
    #pragma unroll
    for(int u=0;u<4;++u){
      vx[u]=(lane>>3)*2048 + 16*((lane&7) ^ (((lane>>4)&3)|((u&1)<<2))) + u*(8*2048-1024);
      const int X=((((lane>>4)&1)^(u>>1))<<1);
      vw[u]=((lane>>2)&7)*8192 + (lane>>5)*64 + 16*((lane&3)^X) + (u>>1)*(8*8192) + (u&1)*128 - u*1024 + 4096;
    }
    auto issue1=[&](int s){
      const int st=(s+rot)&15; LAS char* d=L+(s&3)*SLOT+lw*4096;
      const int sx=lw*(4*8*2048)+st*128, sw=(st*64+lw*16)*8192;
      #pragma unroll
      for(int u=0;u<4;++u) DMA(rx,d,vx[u],sx,u,0);
      #pragma unroll
      for(int u=0;u<4;++u) DMA(rw,d+16384,vw[u],sw,u,0);
    };
    issue1(0); issue1(1);
    WAITV(8); BAR();
    issue1(2); issue1(3);
    const __amdgpu_buffer_rsrc_t rk=__builtin_amdgcn_make_buffer_rsrc((void*)((br?P.k1:P.k0)+(size_t)(mb*4*32+head)*4096),0,3*32*8192+8192,0x00020000);
    const int vk=(lane>>4)*256 + 16*((lane&15) ^ (((lane>>4)<<2)|lw));
    v4u Kr[8];
    #pragma unroll
    for(int u=0;u<8;++u) asm volatile("":"=v"(Kr[u]));
    const int rK=4+2*(j&3);
    const u64 myn=*(volatile LAS u64*)(L+NLDS);
    int c_st=(4+rot)&15; unsigned c_d=lb+lw*4096;
    int c_sx=lw*(4*8*2048)+c_st*128, c_sw=(c_st*64+lw*16)*8192;
    #pragma unroll 1
    for(int s=1;s<13;++s){
      WAITV(16); BAR();
      #pragma unroll
      for(int u=0;u<4;++u) DMA(rx,(LAS char*)(size_t)c_d,vx[u],c_sx,u,0);
      __builtin_amdgcn_sched_barrier(0);
      const unsigned w_d=c_d+16384; const int w_s=c_sw;
      c_st=(c_st+1)&15; c_d=lb+lw*4096+((s+4)&3)*SLOT;
      c_sx=lw*(4*8*2048)+c_st*128; c_sw=(c_st*64+lw*16)*8192;
      asm volatile("":"+s"(c_d),"+s"(c_sx),"+s"(c_sw));
      __builtin_amdgcn_sched_barrier(0);
      #pragma unroll
      for(int u=0;u<4;++u) DMA(rw,(LAS char*)(size_t)w_d,vw[u],w_s,u,0);
      __builtin_amdgcn_sched_barrier(0);
      if(s==rK){
        #pragma unroll
        for(int u=0;u<8;++u){ int so=(u>>1)*32*8192+(lw+4*(u&1))*1024; asm volatile("buffer_load_dwordx4 %0, %1, %2, %3 offen nt":"+v"(Kr[u]):"v"(vk),"s"(rk),"s"(so):"memory"); }
      }
    }
    const __amdgpu_buffer_rsrc_t rwo=__builtin_amdgcn_make_buffer_rsrc((void*)((br?P.wo1:P.wo0)+(size_t)oct*1024*1024+jj*128-2048),0,1024*2048-jj*256+4096,0x00020000);
    const __amdgpu_buffer_rsrc_t rv=__builtin_amdgcn_make_buffer_rsrc((void*)((br?P.v1:P.v0)+(size_t)(mb*4*32+oct*8)*4096),0,3*32*8192+8*8192,0x00020000);
    const __amdgpu_buffer_rsrc_t rp=__builtin_amdgcn_make_buffer_rsrc((void*)(P.ws+P_OFF+((size_t)(br*4+mb)*32+oct*8)*8192),0,8*8192,0x00020000);
    int vw2[4], vv[4];
    #pragma unroll
    for(int u=0;u<4;++u){
      const int X=((((lane>>4)&1)^(u>>1))<<1);
      vw2[u]=((lane>>2)&7)*2048 + (lane>>5)*64 + 16*((lane&3)^X) + (u>>1)*(8*2048) + (u&1)*128 - u*1024 + 4096;
      vv[u]=(lane>>3)*256 + 16*((lane&7) ^ (((lane>>4)&3)|((u&1)<<2))) + u*(8*256-1024);
    }
    auto hseq=[&](int t)->int{
      if(t==0) return jj;
      int e=(t-1)+2*mb; if(e>=7) e-=7; if(e>=7) e-=7;
      return (jj+1+e)&7;
    };
    auto issue2=[&](int r){
      const int h=hseq(r>>1); LAS char* d=L+((r+2)&3)*SLOT+lw*4096;
      const int sv=lw*(32*8192)+h*8192+(r&1)*128, sw=((2*h+(r&1))*64+lw*16)*2048;
      #pragma unroll
      for(int u=0;u<4;++u) DMA(rv,d,vv[u],sv,u,0);
      #pragma unroll
      for(int u=0;u<4;++u) DMA(rwo,d+16384,vw2[u],sw,u,0);
    };
    auto issue_wo=[&](int r){
      const int h=hseq(r>>1); LAS char* d=L+((r+2)&3)*SLOT+lw*4096; const int sw=((2*h+(r&1))*64+lw*16)*2048;
      #pragma unroll
      for(int u=0;u<4;++u) DMA(rwo,d+16384,vw2[u],sw,u,0);
    };
    auto issue_v=[&](int r){
      const int h=hseq(r>>1); LAS char* d=L+((r+2)&3)*SLOT+lw*4096; const int sv=lw*(32*8192)+h*8192+(r&1)*128;
      #pragma unroll
      for(int u=0;u<4;++u) DMA(rv,d,vv[u],sv,u,0);
    };
    const unsigned kva=lb+lw*1024+lane*16;
    WAITV(16); BAR();
    #pragma unroll
    for(int u=0;u<4;++u) WR128(kva,(u>>1)*8192+(u&1)*4096,Kr[u]);
    issue_wo(2);
    WAITV(12); BAR();
    #pragma unroll
    for(int u=4;u<8;++u) WR128(kva,SLOT+((u-4)>>1)*8192+(u&1)*4096,Kr[u]);
    issue_wo(3);
    WAITV(8); WAITL(0); BAR();
    issue2(0);
    BAR();
    BAR();
    issue2(1);
    WAITV(8); BAR();
    issue_v(2); issue_v(3);
    WAITV(8); BAR();
    gu64* inbox=(gu64*)(ctl+PIN_OFF+b*256+(lane&31)*8);
    unsigned ready=0xFu<<(4*jj);
    u64 pv=~myn;
    auto need_head=[&](int h){
      asm volatile("":"+v"(pv));
      ready|=(unsigned)__ballot(pv==myn);
      for(unsigned spins=0; ((ready>>(4*h))&15u)!=15u && spins<SPIN_CAP; ++spins){
        u64 v=__hip_atomic_load(inbox,RLX_AGENT);
        ready|=(unsigned)__ballot(v==myn);
        if(((ready>>(4*h))&15u)!=15u) __builtin_amdgcn_s_sleep(1);
      }
      if(ready!=0xFFFFFFFFu) asm volatile("global_load_dwordx2 %0, %1, off sc1":"+v"(pv):"v"(inbox):"memory");
    };
    auto fetch_p=[&](int t){
      const int h=hseq(t);
      need_head(h);
      LAS char* d=L+PBUF+(t&1)*8192+lw*2048;
      DMA(rp,d,lane*16,h*8192+lw*2048,0,SC1); DMA(rp,d,lane*16,h*8192+lw*2048,1,SC1);
    };
    #pragma unroll 1
    for(int s=1;s<13;++s){
      if(s==1) asm volatile("global_load_dwordx2 %0, %1, off sc1":"+v"(pv):"v"(inbox):"memory");
      if(s==2) asm volatile("s_waitcnt vmcnt(8)":"+v"(pv)::"memory");
      if(!(s&1)) fetch_p(s>>1);
      issue2(s+3); if(s==1) WAITV(13); else WAITV(16);
      BAR();
    }
    WAITV(8); BAR();
    fetch_p(7);
    {
      const int pj=(lane>>2)&7;
      u64 fresh;
      { gu64* pp=(gu64*)(ctl+R_OFF+(g8+8*(mb*8+pj))*128); asm volatile("global_load_dwordx2 %0, %1, off sc1":"=v"(fresh):"v"(pp):"memory"); }
      WAITV(3); BAR();
      asm volatile("s_waitcnt vmcnt(0)":"+v"(fresh)::"memory");
      BAR();
      if(lw==0 && lane<32 && pj!=jj) __hip_atomic_store((gu64*)(ctl+PIN_OFF+(g8+8*(mb*8+pj))*256+(jj*4+(lane&3))*8),fresh,RLX_AGENT);
    }
    return;
  }
  __builtin_amdgcn_s_setprio(2);
  const unsigned lb=(unsigned)(size_t)(LAS char*)smem;
  const int g=lane>>4, i16=lane&15, q=i16>>2, p=lane&3;
  const int wm=w>>1, wn=w&1;
  const int pid = (lane<8) ? (g8+8*(mb*8+lane)) : ((br*4+(lane&3))+8*j);
  unsigned xa[4], wc[2][2];
  {
    const int s=(i16>>1)&7;
    #pragma unroll
    for(int kk=0;kk<2;++kk)
    #pragma unroll
    for(int hf=0;hf<2;++hf) xa[kk*2+hf]=lb+128*(wm*64+32*(wn^hf)+i16)+16*((4*kk+g)^s);
  }
  #pragma unroll
  for(int c1=0;c1<2;++c1)
  #pragma unroll
  for(int t=0;t<2;++t){ int X=((t^g)&1)<<1; wc[c1][t]=lb+16384+2048*g+64*(4*t+q)+16*((2*c1+(p>>1))^X)+8*(p&1)+1024*wn; }
  unsigned ka[2][2], sca;
  {
    const int fk=((i16&3)<<2)|((i16>>2)&3);
    #pragma unroll
    for(int mm=0;mm<2;++mm)
    #pragma unroll
    for(int t=0;t<2;++t) ka[mm][t]=lb+wm*SLOT+wn*8192+256*i16+16*(((2*wn+mm)*4+2*t+(g>>1))^fk)+8*(g&1);
    sca=lb+SCR+w*4096+lane*16;
    asm volatile("":"+v"(ka[0][0]),"+v"(ka[0][1]),"+v"(ka[1][0]),"+v"(ka[1][1]),"+v"(sca));
  }
  f32x4 acc[4][4];
  #pragma unroll
  for(int m=0;m<4;++m)
  #pragma unroll
  for(int n=0;n<4;++n) acc[m][n]=(f32x4){0,0,0,0};
  unsigned tsink=0;
  if(w==2){
    const GAS char* a;
    if(lane<32) a=(const GAS char*)((br?P.wo1:P.wo0)+(size_t)oct*1024*1024)+lane*65536;
    else if(lane<36) a=(const GAS char*)((br?P.v1:P.v0)+(size_t)((mb*4+(lane&3))*32+oct*8)*4096);
    else if(lane<40) a=(const GAS char*)((br?P.k1:P.k0)+(size_t)((mb*4+(lane&3))*32+head)*4096);
    else if(lane<48) a=(const GAS char*)(P.out+(size_t)(mb*128+(lane&7)*16)*2048);
    else if(lane<50) a=(const GAS char*)(P.ws+PART_OFF+(size_t)((br*4+mb)*8+jj)*(4*4*4*2048))+(lane-48)*65536;
    else a=(const GAS char*)(P.ws+P_OFF+((size_t)(br*4+mb)*32+oct*8)*8192);
    if(lane<51) asm volatile("global_load_dword %0, %1, off":"+v"(tsink):"v"(a):"memory");
  }
  if(w==0){
    gu64* rp_=(gu64*)(ctl+R_OFF+b*128);
    u64 old=__hip_atomic_load(rp_,RLX_AGENT);
    u64 n=old*0x9E3779B97F4A7C15ull+0x632BE59BD9B4E019ull;
    if(lane==0){ __hip_atomic_store(rp_,n,RLX_AGENT); *(volatile LAS u64*)((LAS char*)smem+NLDS)=n; }
    WAITL(0);
  }
  BAR();
  const u64 myn=*(volatile LAS u64*)((LAS char*)smem+NLDS);
  gemm_loop1(acc,xa,wc);
  u64 pn;
  { gu64* pp=(gu64*)(ctl+R_OFF+pid*128); asm volatile("global_load_dwordx2 %0, %1, off sc1":"=v"(pn):"v"(pp):"memory"); }
  v2u Kf[2][2][2][2];
  {
    #pragma unroll
    for(int l=1;l>=0;--l)
    #pragma unroll
    for(int mm=0;mm<2;++mm)
    #pragma unroll
    for(int t=0;t<2;++t){
      const unsigned a=ka[mm][t]^(l*8192);
      RD64(Kf[l][0][mm][t],a,0); RD64(Kf[l][1][mm][t],a,4096);
    }
  }
  #pragma unroll
  for(int m=0;m<4;++m) FENCE4("",acc[m][0],acc[m][1],acc[m][2],acc[m][3]);
  v4u QB[4][2];
  #pragma unroll
  for(int n=3;n>=0;--n)
  #pragma unroll
  for(int mm=0;mm<2;++mm){
    QB[n][mm]=(v4u){pk(acc[2*mm][n][0],acc[2*mm][n][1]),pk(acc[2*mm][n][2],acc[2*mm][n][3]),pk(acc[2*mm+1][n][0],acc[2*mm+1][n][1]),pk(acc[2*mm+1][n][2],acc[2*mm+1][n][3])};
  }
  WAITL(0);
  FENCE4("",QB[0][0],QB[0][1],QB[1][0],QB[1][1]);
  FENCE4("s_nop 1",QB[2][0],QB[2][1],QB[3][0],QB[3][1]);
  f32x4 S[2][2][2];
  #pragma unroll
  for(int kb=0;kb<2;++kb)
  #pragma unroll
  for(int qb=0;qb<2;++qb) M16Z(S[1][kb][qb],cat(Kf[1][kb][0][0],Kf[1][kb][0][1]),QB[2+qb][0]);
  #pragma unroll
  for(int kb=0;kb<2;++kb)
  #pragma unroll
  for(int qb=0;qb<2;++qb) M16(S[1][kb][qb],cat(Kf[1][kb][1][0],Kf[1][kb][1][1]),QB[2+qb][1]);
  #pragma unroll
  for(int kb=0;kb<2;++kb)
  #pragma unroll
  for(int qb=0;qb<2;++qb) M16Z(S[0][kb][qb],cat(Kf[0][kb][0][0],Kf[0][kb][0][1]),QB[qb][0]);
  FENCE4("s_nop 3",S[1][0][0],S[1][0][1],S[1][1][0],S[1][1][1]);
  {
    #pragma unroll
    for(int kb=0;kb<2;++kb)
    #pragma unroll
    for(int qb=0;qb<2;++qb) WR128(sca,(kb*2+qb)*1024,S[1][kb][qb]);
  }
  #pragma unroll
  for(int kb=0;kb<2;++kb)
  #pragma unroll
  for(int qb=0;qb<2;++qb) M16(S[0][kb][qb],cat(Kf[0][kb][1][0],Kf[0][kb][1][1]),QB[qb][1]);
  WAITL(0);
  BAR();
  f32x4 T[2][2];
  {
    const unsigned sc=sca^4096;
    #pragma unroll
    for(int kb=0;kb<2;++kb)
    #pragma unroll
    for(int qb=0;qb<2;++qb) RD128(T[kb][qb],sc,(kb*2+qb)*1024);
    WAITL(0);
    FENCE4("",T[0][0],T[0][1],T[1][0],T[1][1]);
    FENCE4("",S[0][0][0],S[0][0][1],S[0][1][0],S[0][1][1]);
    #pragma unroll
    for(int kb=0;kb<2;++kb)
    #pragma unroll
    for(int qb=0;qb<2;++qb) T[kb][qb]+=S[0][kb][qb];
  }
  v4u PB[2];
  constexpr float SCL=0.08838834764831845f*1.4426950408889634f;
  {
    float mx[2], sum[2], e[2][8];
    #pragma unroll
    for(int qb=0;qb<2;++qb)
      mx[qb]=fmaxf(fmaxf(fmaxf(T[0][qb][0],T[0][qb][1]),fmaxf(T[0][qb][2],T[0][qb][3])),fmaxf(fmaxf(T[1][qb][0],T[1][qb][1]),fmaxf(T[1][qb][2],T[1][qb][3])));
    #pragma unroll
    for(int qb=0;qb<2;++qb){ auto r=__builtin_amdgcn_permlane16_swap(__float_as_uint(mx[qb]),__float_as_uint(mx[qb]),false,false); mx[qb]=fmaxf(__uint_as_float(r[0]),__uint_as_float(r[1])); }
    #pragma unroll
    for(int qb=0;qb<2;++qb){ auto r=__builtin_amdgcn_permlane32_swap(__float_as_uint(mx[qb]),__float_as_uint(mx[qb]),false,false); mx[qb]=fmaxf(__uint_as_float(r[0]),__uint_as_float(r[1])); }
    #pragma unroll
    for(int qb=0;qb<2;++qb){
      const float nm=-mx[qb]*SCL; float s4[2];
      #pragma unroll
      for(int kb=0;kb<2;++kb){
        #pragma unroll
        for(int c=0;c<4;++c) e[qb][kb*4+c]=__builtin_amdgcn_exp2f(__builtin_fmaf(T[kb][qb][c],SCL,nm));
        s4[kb]=(e[qb][kb*4]+e[qb][kb*4+1])+(e[qb][kb*4+2]+e[qb][kb*4+3]);
      }
      sum[qb]=s4[0]+s4[1];
    }
    #pragma unroll
    for(int qb=0;qb<2;++qb){ auto r=__builtin_amdgcn_permlane16_swap(__float_as_uint(sum[qb]),__float_as_uint(sum[qb]),false,false); sum[qb]=__uint_as_float(r[0])+__uint_as_float(r[1]); }
    #pragma unroll
    for(int qb=0;qb<2;++qb){ auto r=__builtin_amdgcn_permlane32_swap(__float_as_uint(sum[qb]),__float_as_uint(sum[qb]),false,false); sum[qb]=__uint_as_float(r[0])+__uint_as_float(r[1]); }
    #pragma unroll
    for(int qb=0;qb<2;++qb){
      const float inv=__builtin_amdgcn_rcpf(sum[qb]);
      PB[qb]=(v4u){pk(e[qb][0]*inv,e[qb][1]*inv),pk(e[qb][2]*inv,e[qb][3]*inv),pk(e[qb][4]*inv,e[qb][5]*inv),pk(e[qb][6]*inv,e[qb][7]*inv)};
    }
  }
  {
    const __amdgpu_buffer_rsrc_t rs=__builtin_amdgcn_make_buffer_rsrc((void*)(P.ws+P_OFF+((size_t)(br*4+mb)*32+head)*8192),0,8192,0x00020000);
    #pragma unroll
    for(int qb=0;qb<2;++qb){
      __builtin_amdgcn_raw_buffer_store_b128(PB[qb],rs,lane*16,(2*w+qb)*1024,SC1);
      WR128(lb+PBUF+w*2048+lane*16,qb*1024,PB[qb]);
    }
  }
  unsigned pa[4];
  {
    const int s=(i16>>1)&7;
    #pragma unroll
    for(int kk=0;kk<2;++kk)
    #pragma unroll
    for(int hf=0;hf<2;++hf) xa[kk*2+hf]=lb+2*SLOT+128*(wm*64+32*(hf^(oct>>1))+i16)+16*((4*kk+g)^s);
    #pragma unroll
    for(int n=0;n<4;++n) pa[n]=lb+PBUF+(4*wm+(n^oct))*1024+lane*16;
  }
  #pragma unroll
  for(int c1=0;c1<2;++c1)
  #pragma unroll
  for(int t=0;t<2;++t){ int X=((t^g)&1)<<1; wc[c1][t]=lb+2*SLOT+16384+2048*g+64*(4*t+q)+16*((2*c1+(p>>1))^X)+8*(p&1)+1024*wn; }
  #pragma unroll
  for(int m=0;m<4;++m)
  #pragma unroll
  for(int n=0;n<4;++n) acc[m][n]=(f32x4){0,0,0,0};
  WAITL(0);
  asm volatile("s_waitcnt vmcnt(0)":"+v"(pn),"+v"(tsink)::"memory");
  if((lane<8)&&(lane!=jj)) __hip_atomic_store((gu64*)(ctl+PIN_OFF+pid*256+(jj*4+w)*8),pn,RLX_AGENT);
  BAR();
  {
    f32x4 aw[4][4];
    v2u A[2][4][2]; v4u B[2][4];
    #pragma unroll
    for(int n=0;n<4;++n) rdB<0>(B[0][n],xa,n,0);
    #pragma unroll
    for(int m=0;m<4;++m){ rdA<0,0>(A[0][m][0],wc,m,0); rdA<0,0>(A[0][m][1],wc,m,1); }
    #pragma unroll 1
    for(int t=0;t<8;++t) head_step(acc,aw,A,B,xa,wc,pa);
    WAITL(0);
  }
  FENCE4("s_nop 7",acc[3][0],acc[3][1],acc[3][2],acc[3][3]);
  #pragma unroll
  for(int m=0;m<3;++m) FENCE4("",acc[m][0],acc[m][1],acc[m][2],acc[m][3]);
  {
    const int rgrp=(br*4+mb)*8+jj;
    const __amdgpu_buffer_rsrc_t rp=__builtin_amdgcn_make_buffer_rsrc((void*)(P.ws+PART_OFF+(size_t)rgrp*(4*4*4*2048)),0,4*4*4*2048,0x00020000);
    #pragma unroll
    for(int n=1;n<4;++n){
      #pragma unroll
      for(int m=0;m<4;m+=2){
        v4u o=(v4u){pk(acc[m][n][0],acc[m][n][1]),pk(acc[m][n][2],acc[m][n][3]),pk(acc[m+1][n][0],acc[m+1][n][1]),pk(acc[m+1][n][2],acc[m+1][n][3])};
        __builtin_amdgcn_raw_buffer_store_b128(o,rp,lane*16,(((n^oct)*4+oct)*4+w)*2048+m*512,SC1);
      }
    }
    WAITV(0);
    const bool rpart=(lane>=8)&&(lane<12)&&((lane&3)!=oct);
    gu64* rflag=(gu64*)(ctl+RIN_OFF+pid*128+(oct*4+w)*8);
    if(rpart) __hip_atomic_store(rflag,pn,RLX_AGENT);
    poll_eq((gu64*)(ctl+RIN_OFF+b*128+((lane&3)*4+w)*8),myn,(lane<4)&&(lane!=oct));
    const u64 fresh=__hip_atomic_load((gu64*)(ctl+R_OFF+pid*128),RLX_AGENT);
    v4u in[3][2];
    #pragma unroll
    for(int e=1;e<4;++e){
      #pragma unroll
      for(int m=0;m<2;++m) in[e-1][m]=__builtin_amdgcn_raw_buffer_load_b128(rp,lane*16,((oct*4+(e^oct))*4+w)*2048+m*1024,SC1);
    }
    #pragma unroll
    for(int e=0;e<3;++e){
      #pragma unroll
      for(int m=0;m<2;++m){
        acc[2*m][0][0]+=bflo(in[e][m][0]); acc[2*m][0][1]+=bfhi(in[e][m][0]); acc[2*m][0][2]+=bflo(in[e][m][1]); acc[2*m][0][3]+=bfhi(in[e][m][1]);
        acc[2*m+1][0][0]+=bflo(in[e][m][2]); acc[2*m+1][0][1]+=bfhi(in[e][m][2]); acc[2*m+1][0][2]+=bflo(in[e][m][3]); acc[2*m+1][0][3]+=bfhi(in[e][m][3]);
      }
    }
    if(rpart) __hip_atomic_store(rflag,fresh,RLX_AGENT);
    u16* op=P.out+(size_t)(mb*128+wm*64+16*oct+i16)*2048+br*1024+jj*128+wn*64+4*g;
    #pragma unroll
    for(int m=0;m<4;++m) *(GAS v2u*)(op+16*m)=(v2u){pk(acc[m][0][0],acc[m][0][1]),pk(acc[m][0][2],acc[m][0][3])};
  }
}

extern "C" void kernel_launch(void* const* d_in, const int* in_sizes, int n_in,
                              void* d_out, int out_size, void* d_ws, size_t ws_size,
                              hipStream_t stream) {
  Ptrs P;
  P.x0=(const u16*)d_in[0]; P.k0=(const u16*)d_in[1]; P.v0=(const u16*)d_in[2]; P.wq0=(const u16*)d_in[3]; P.wo0=(const u16*)d_in[4];
  P.x1=(const u16*)d_in[5]; P.k1=(const u16*)d_in[6]; P.v1=(const u16*)d_in[7]; P.wq1=(const u16*)d_in[8]; P.wo1=(const u16*)d_in[9];
  P.out=(u16*)d_out; P.ws=(char*)d_ws;
  P.ws=(char*)d_ws+((ws_size-((size_t)18<<20))&~(size_t)((2u<<20)-1));
  const int lds=LDS_BYTES;
  (void)hipFuncSetAttribute((const void*)k_fused,hipFuncAttributeMaxDynamicSharedMemorySize,lds);
  k_fused<<<256,512,lds,stream>>>(P);
}
```
